```python
import torch

def _softmax(x, axis=-1):
    return torch.softmax(x, dim=axis)
def _gelu(x):
    return torch.nn.functional.gelu(x, approximate="tanh")
def _silu(x):
    return torch.nn.functional.silu(x)
def _sigmoid(x):
    return torch.sigmoid(x)
def _layernorm(x, axis=-1, eps=1e-5):
    m = x.mean(dim=axis, keepdim=True)
    v = x.var(dim=axis, keepdim=True, unbiased=False)
    return (x - m) / torch.sqrt(v + eps)
def _scatter_add(tgt, idx, src, axis=0):
    return tgt.clone().index_add_(axis, idx, src)
def _segment_sum(src, seg, n):
    out = torch.zeros((n,) + src.shape[1:], dtype=src.dtype)
    return out.index_add_(0, seg, src)
def _pad(x, pw):
    flat = tuple(v for pair in reversed(pw) for v in pair)
    return torch.nn.functional.pad(x, flat)

kernel_name = 'gated_mix__sliding_block_cached__gqa_block_cached__decode'

def setup_inputs():

    _w = (lambda a: (lambda t: t.to(torch.bfloat16) if t.is_floating_point() else t)(torch.as_tensor(a)))
    def _A_setup():
        B, Sq, D = 2, 1, 1024
        Hq, Hkv, Sk, Dh = 32, 4, 32768, 128
        assert Hq % Hkv == 0, (Hq, Hkv)
        return {
            "x": _w((lambda *s: torch.randn(*s, dtype=torch.bfloat16) * 0.05)(B, Sq, D)),
            "k_cache": _w((lambda *s: torch.randn(*s, dtype=torch.bfloat16) * 6.4)(B, Hkv, Sk, Dh)),
            "v_cache": _w((lambda *s: torch.randn(*s, dtype=torch.bfloat16) * 0.05)(B, Hkv, Sk, Dh)),
            "wq": _w((lambda *s: torch.randn(*s, dtype=torch.bfloat16) * 6.4)(D, Hq * Dh) * (D ** -0.5)),
            "wo": _w((lambda *s: torch.randn(*s, dtype=torch.bfloat16) * 0.05)(Hq * Dh, D) * ((Hq * Dh) ** -0.5)),
        }
    _A_in = _A_setup()

    def _B_setup():
        B, Sq, D = 2, 1, 1024
        Hq, Hkv, Sk, Dh = 32, 4, 32768, 128
        assert Hq % Hkv == 0, (Hq, Hkv)
        return {
            "x": _w((lambda *s: torch.randn(*s, dtype=torch.bfloat16) * 0.05)(B, Sq, D)),
            "k_cache": _w((lambda *s: torch.randn(*s, dtype=torch.bfloat16) * 6.4)(B, Hkv, Sk, Dh)),
            "v_cache": _w((lambda *s: torch.randn(*s, dtype=torch.bfloat16) * 0.05)(B, Hkv, Sk, Dh)),
            "wq": _w((lambda *s: torch.randn(*s, dtype=torch.bfloat16) * 6.4)(D, Hq * Dh) * (D ** -0.5)),
            "wo": _w((lambda *s: torch.randn(*s, dtype=torch.bfloat16) * 0.05)(Hq * Dh, D) * ((Hq * Dh) ** -0.5)),
        }
    _B_in = _B_setup()

    return {
        "A_x": _A_in["x"],
        "A_k_cache": _A_in["k_cache"],
        "A_v_cache": _A_in["v_cache"],
        "A_wq": _A_in["wq"],
        "A_wo": _A_in["wo"],
        "B_x": _B_in["x"],
        "B_k_cache": _B_in["k_cache"],
        "B_v_cache": _B_in["v_cache"],
        "B_wq": _B_in["wq"],
        "B_wo": _B_in["wo"],
        "gate_w": _w((lambda *s: torch.randn(*s, dtype=torch.bfloat16) * 0.05)(1024) * 16.0),
    }

def reference(A_x, A_k_cache, A_v_cache, A_wq, A_wo, B_x, B_k_cache, B_v_cache, B_wq, B_wo, gate_w):
    def _A_ref(x, k_cache, v_cache, wq, wo):
        B, Sq, D = x.shape
        Hq, Dh = 32, 128
        Hkv, Sk = k_cache.shape[1], k_cache.shape[2]
        W = min(4096, max(256, Sk // 4))
        rep = Hq // Hkv
        q = (lambda x, *d: x.permute(*d))((x @ wq).reshape(B, Sq, Hq, Dh), 0, 2, 1, 3)
        k = (lambda x, n, axis: torch.repeat_interleave(x, n, dim=axis))(k_cache[:, :, Sk - W:, :], rep, axis=1)
        v = (lambda x, n, axis: torch.repeat_interleave(x, n, dim=axis))(v_cache[:, :, Sk - W:, :], rep, axis=1)
        scores = torch.einsum("bhqd,bhkd->bhqk", q, k) * (Dh ** -0.5)
        p = _softmax(scores, axis=-1)
        o = torch.einsum("bhqk,bhkd->bhqd", p, v)
        return (lambda x, *d: x.permute(*d))(o, 0, 2, 1, 3).reshape(B, Sq, Hq * Dh) @ wo

    def _B_ref(x, k_cache, v_cache, wq, wo):
        B, Sq, D = x.shape
        Hq, Dh = 32, 128
        Hkv = k_cache.shape[1]
        rep = Hq // Hkv
        q = (lambda x, *d: x.permute(*d))((x @ wq).reshape(B, Sq, Hq, Dh), 0, 2, 1, 3)
        k = (lambda x, n, axis: torch.repeat_interleave(x, n, dim=axis))(k_cache, rep, axis=1)
        v = (lambda x, n, axis: torch.repeat_interleave(x, n, dim=axis))(v_cache, rep, axis=1)
        scores = torch.einsum("bhqd,bhkd->bhqk", q, k) * (Dh ** -0.5)
        p = _softmax(scores, axis=-1)
        o = torch.einsum("bhqk,bhkd->bhqd", p, v)
        return (lambda x, *d: x.permute(*d))(o, 0, 2, 1, 3).reshape(B, Sq, Hq * Dh) @ wo

    x = A_x
    g = _sigmoid((x * gate_w).sum(-1, keepdims=True))
    a = _A_ref(x, A_k_cache, A_v_cache, A_wq, A_wo)
    b = _B_ref(x, B_k_cache, B_v_cache, B_wq, B_wo)
    return g * a + (1 - g) * b
```

```cpp
#include <hip/hip_runtime.h>
#include <hip/hip_bf16.h>

#define KV_AUX 0
#define W_AUX 0
#define PRE 2
#define LOCKSTEP 1
#define STAMPS 0

namespace {
typedef unsigned v4u __attribute__((ext_vector_type(4)));
typedef unsigned v2u __attribute__((ext_vector_type(2)));
typedef float f32x4 __attribute__((ext_vector_type(4)));
typedef short bf16x8 __attribute__((ext_vector_type(8)));
typedef short bf16x4 __attribute__((ext_vector_type(4)));
typedef unsigned long long u64;
#define LAS __attribute__((address_space(3)))
typedef __amdgpu_buffer_rsrc_t rsrc_t;

constexpr int NWG = 256, NTHR = 512;
constexpr int SK = 32768, WIN = 4096;
constexpr int SC1 = 16;
constexpr int RW3 = 0x00020000;
constexpr int WS_EPOCH = 0;
constexpr int WS_QP = 4096;
constexpr int WS_INBOX = WS_QP + 262144;
constexpr int IB_O = 0, IB_ML = 16384, IB_T3 = 18432, INBOX_B = 34816;
constexpr int WS_END = WS_INBOX + NWG * INBOX_B;
constexpr int WS_STAMP = WS_END;
constexpr int WS_BYTES = WS_STAMP + NWG * 8 * 16 * 8;
constexpr int L_XQ = 0;
constexpr int L_Q = L_XQ + 64 * 144;
constexpr int L_MA = L_Q + 16 * 272;
constexpr int L_MB = L_MA + 256 * 144;
constexpr int L_MLA = L_MB + 256 * 144;
constexpr int L_MLB = L_MLA + 512;
constexpr int L_T2 = L_MLB + 512;
constexpr int L_T2ML = L_T2 + 1024;
constexpr int L_GATE = L_T2ML + 64;
constexpr int L_T3 = L_GATE + 16;
constexpr int L_STAMP = L_T3 + 256;
constexpr int L_END = L_STAMP + (STAMPS ? 1024 : 0);
constexpr float CS = 0.08838834764831845f * 1.4426950408889634f;
constexpr unsigned SPIN_CAP = 1u << 16;

struct Params {
  const void* x; const void* ak; const void* av; const void* wqa; const void* woa;
  const void* bk; const void* bv; const void* wqb; const void* wob; const void* gw;
  void* out; void* ws;
};

__device__ __forceinline__ rsrc_t mk(const void* p, unsigned bytes) { return __builtin_amdgcn_make_buffer_rsrc((void*)p, 0, (int)bytes, RW3); }
template <int AUX> __device__ __forceinline__ v4u ld16(rsrc_t r, int voff, int soff) { return __builtin_amdgcn_raw_buffer_load_b128(r, voff, soff, AUX); }
__device__ __forceinline__ void st16(rsrc_t r, v4u v, int voff, int soff) { __builtin_amdgcn_raw_buffer_store_b128(v, r, voff, soff, SC1); }
__device__ __forceinline__ f32x4 mfma32(v4u a, v4u b, f32x4 c) {
  return __builtin_amdgcn_mfma_f32_16x16x32_bf16(__builtin_bit_cast(bf16x8, a), __builtin_bit_cast(bf16x8, b), c, 0, 0, 0);
}
__device__ __forceinline__ f32x4 mfma16(v2u a, v2u b, f32x4 c) {
  return __builtin_amdgcn_mfma_f32_16x16x16bf16_1k(__builtin_bit_cast(bf16x4, a), __builtin_bit_cast(bf16x4, b), c, 0, 0, 0);
}
__device__ __forceinline__ unsigned permlo(unsigned hi, unsigned lo) { return __builtin_amdgcn_perm(hi, lo, 0x05040100u); }
__device__ __forceinline__ unsigned permhi(unsigned hi, unsigned lo) { return __builtin_amdgcn_perm(hi, lo, 0x07060302u); }
__device__ __forceinline__ v4u col8(const v4u& r0, const v4u& r1, const v4u& r2, const v4u& r3, const v4u& r4, const v4u& r5, const v4u& r6, const v4u& r7, int t) {
  const int d = t >> 1;
  v4u o;
  if (t & 1) { o.x = permhi(r1[d], r0[d]); o.y = permhi(r3[d], r2[d]); o.z = permhi(r5[d], r4[d]); o.w = permhi(r7[d], r6[d]); }
  else       { o.x = permlo(r1[d], r0[d]); o.y = permlo(r3[d], r2[d]); o.z = permlo(r5[d], r4[d]); o.w = permlo(r7[d], r6[d]); }
  return o;
}
__device__ __forceinline__ v2u col4(const v4u& r0, const v4u& r1, const v4u& r2, const v4u& r3, int t) {
  const int d = t >> 1;
  v2u o;
  if (t & 1) { o.x = permhi(r1[d], r0[d]); o.y = permhi(r3[d], r2[d]); }
  else       { o.x = permlo(r1[d], r0[d]); o.y = permlo(r3[d], r2[d]); }
  return o;
}
__device__ __forceinline__ unsigned cvtpk(float lo, float hi) {
  typedef __bf16 b2 __attribute__((ext_vector_type(2)));
  typedef float f2 __attribute__((ext_vector_type(2)));
  f2 f = {lo, hi};
  return __builtin_bit_cast(unsigned, __builtin_convertvector(f, b2));
}
template <int CTRL> __device__ __forceinline__ float dpp(float x) {
  return __builtin_bit_cast(float, __builtin_amdgcn_mov_dpp(__builtin_bit_cast(int, x), CTRL, 0xf, 0xf, true));
}
constexpr int XOR1 = 0xB1, XOR2 = 0x4E, ROR4 = 0x124, ROR8 = 0x128;
__device__ __forceinline__ float x16_max(float x) {
  auto s = __builtin_amdgcn_permlane16_swap(__float_as_uint(x), __float_as_uint(x), false, false);
  x = fmaxf(__uint_as_float(s[0]), __uint_as_float(s[1]));
  auto t = __builtin_amdgcn_permlane32_swap(__float_as_uint(x), __float_as_uint(x), false, false);
  return fmaxf(__uint_as_float(t[0]), __uint_as_float(t[1]));
}
__device__ __forceinline__ float x32_sum(float x) {
  auto t = __builtin_amdgcn_permlane32_swap(__float_as_uint(x), __float_as_uint(x), false, false);
  return __uint_as_float(t[0]) + __uint_as_float(t[1]);
}
__device__ __forceinline__ float x16_sum(float x) {
  auto s = __builtin_amdgcn_permlane16_swap(__float_as_uint(x), __float_as_uint(x), false, false);
  return x32_sum(__uint_as_float(s[0]) + __uint_as_float(s[1]));
}
__device__ __forceinline__ float ex2(float x) { return __builtin_amdgcn_exp2f(x); }
struct Tag { unsigned lo, hi; };
__device__ __forceinline__ unsigned enc(float f, Tag t) { return ((__float_as_uint(f) + 0x800u) >> 12) | t.lo; }
__device__ __forceinline__ v4u gran2(float a, float b, Tag t) { return v4u{enc(a, t), t.hi, enc(b, t), t.hi}; }
__device__ __forceinline__ v4u gran2raw(unsigned a, unsigned b, Tag t) { return v4u{a | t.lo, t.hi, b | t.lo, t.hi}; }
__device__ __forceinline__ unsigned bad2(v4u v, Tag t) { return ((v.y ^ t.hi) | (v.w ^ t.hi)) | (((v.x ^ t.lo) | (v.z ^ t.lo)) >> 20); }
__device__ __forceinline__ float dec(unsigned lo) { return __uint_as_float(lo << 12); }
#define BAR() asm volatile("s_waitcnt lgkmcnt(0)\n\ts_barrier" ::: "memory")
#define SB() __builtin_amdgcn_sched_barrier(0)
#define MEMFENCE() asm volatile("" ::: "memory")
#define STAMP(i) do {} while (0)
#define COUNT(i, v) do {} while (0)

__global__ void __launch_bounds__(NTHR, 2) mix_decode(Params p) {
  __shared__ __attribute__((aligned(16))) unsigned char smem[L_END];
  LAS unsigned char* L = (LAS unsigned char*)smem;
  const int tid = threadIdx.x, lane = tid & 63, w = __builtin_amdgcn_readfirstlane(tid >> 6);
  const int g = lane >> 4, c16 = lane & 15;
  const int c = blockIdx.x, grp = c & 7, kvh = grp >> 1, b = grp & 1, s = c >> 3;
  const int i = b * 32 + s, idx = kvh * 64 + i;
  STAMP(0);
  const rsrc_t rws = mk(p.ws, WS_BYTES);
  const rsrc_t rx = mk(p.x, 4096);
  const v2u ep = __builtin_amdgcn_raw_buffer_load_b64(rws, WS_EPOCH, 0, SC1);
  const int cg = s >> 1, kh = s & 1;
  const v4u xf = ld16<0>(rx, (c16 & 1) * 2048 + (kh * 512 + 64 * w + 32 * ((c16 >> 1) & 1) + 8 * g) * 2, 0);
  v4u wq[8], wo[8];
  {
    const rsrc_t rq = mk(b ? p.wqb : p.wqa, 1024 * 4096 * 2);
    const int vo = (32 * (c16 >> 3) + 8 * g) * 8192 + (c16 & 7) * 16;
    const int so = (kh * 512 + 64 * w) * 8192 + (kvh * 1024 + cg * 64) * 2;
#pragma unroll
    for (int j = 0; j < 8; ++j) wq[j] = ld16<W_AUX>(rq, vo, so + j * 8192);
  }
  SB();
  BAR();
  const int hc = (s >> 2) & 7, qtc = s & 3;
  const rsrc_t ro = mk(b ? p.wob : p.woa, 4096 * 1024 * 2);
  const int wo_vo = 2 * g * 2048 + c16 * 16;
  const int wo_so = ((kvh * 8 + hc) * 128 + qtc * 32) * 2048 + w * 256;
#pragma unroll
  for (int j = 0; j < 4; ++j) wo[j] = ld16<W_AUX>(ro, wo_vo, wo_so + (8 * (j & 3) + (j >> 2)) * 2048);
  SB();
  Tag tag;
  { const u64 e = (((u64)ep.y << 32) | ep.x) + (1ull << 20); tag.lo = (unsigned)e & 0xFFF00000u; tag.hi = (unsigned)(e >> 32); }
  {
    LAS unsigned char* dst = L + L_XQ + (((w * 2 + (g >> 1)) * 2 + (c16 & 1)) * 2 + (g & 1)) * 144;
    const bool wr = c16 < 4 && ((c16 >> 1) == (g >> 1));
#pragma unroll
    for (int t = 0; t < 8; ++t) {
      const v4u a = col8(wq[0], wq[1], wq[2], wq[3], wq[4], wq[5], wq[6], wq[7], t);
      const f32x4 d = mfma32(a, xf, f32x4{0.f, 0.f, 0.f, 0.f});
      if (wr) *(LAS f32x4*)(dst + t * 16) = d;
    }
  }
  STAMP(1);
  BAR();
  STAMP(12);
  if (w == 0) {
    const int o32 = lane & 31, ph = lane >> 5;
    const int bb = o32 >> 4, half = (o32 >> 3) & 1, t = o32 & 7;
    LAS unsigned char* src = L + L_XQ + ((ph * 8 * 2 + bb) * 2 + half) * 144 + t * 16;
    f32x4 r[8];
#pragma unroll
    for (int k = 0; k < 8; ++k) r[k] = *(LAS f32x4*)(src + k * 576);
    SB();
    f32x4 a = ((r[0] + r[1]) + (r[2] + r[3])) + ((r[4] + r[5]) + (r[6] + r[7]));
#pragma unroll
    for (int k = 0; k < 4; ++k) a[k] = x32_sum(a[k]);
    const int o2 = WS_QP + ((((kvh * 2 + bb) * 2 + b) * 2 + kh) * 16 + cg) * 512 + ph * 256 + (half * 8 + t) * 16;
    st16(rws, gran2(ph ? a.z : a.x, ph ? a.w : a.y, tag), o2, 0);
  }
  SB();
  STAMP(13);
#pragma unroll
  for (int j = 4; j < 8; ++j) wo[j] = ld16<W_AUX>(ro, wo_vo, wo_so + (8 * (j & 3) + (j >> 2)) * 2048);
  SB();
  v4u gx[6];
  if (w == 7) {
    const rsrc_t rg = mk(p.gw, 2048);
    gx[0] = ld16<0>(rx, lane * 16, 0); gx[1] = ld16<0>(rx, lane * 16, 1024);
    gx[2] = ld16<0>(rx, lane * 16, 2048); gx[3] = ld16<0>(rx, lane * 16, 3072);
    gx[4] = ld16<0>(rg, lane * 16, 0); gx[5] = ld16<0>(rg, lane * 16, 1024);
  }
  SB();
  const unsigned gbase = (unsigned)(b * 4 + kvh) * (SK * 256);
  const rsrc_t rak = mk(p.ak, 2u * 4 * SK * 256), rav = mk(p.av, 2u * 4 * SK * 256);
  const rsrc_t rbk = mk(p.bk, 2u * 4 * SK * 256), rbv = mk(p.bv, 2u * 4 * SK * 256);
  const int k_vo = (4 * (c16 & 3) + (c16 >> 2)) * 256 + g * 16;
  const int v_vo = lane * 16;
  const int a_so = gbase + (SK - WIN + s * 128 + w * 16) * 256;
  const int b_so = gbase + (s * 1024 + w * 16) * 256;
  v4u akf[4], avf[4];
#pragma unroll
  for (int j = 0; j < 4; ++j) akf[j] = ld16<KV_AUX>(rak, k_vo + j * 64, a_so);
  SB();
  if (w == 7) {
    float s0 = 0.f, s1 = 0.f;
#pragma unroll
    for (int h = 0; h < 2; ++h)
#pragma unroll
      for (int d = 0; d < 4; ++d) {
        const float g0 = __uint_as_float(gx[4 + h][d] << 16), g1 = __uint_as_float(gx[4 + h][d] & 0xffff0000u);
        s0 = fmaf(__uint_as_float(gx[h][d] << 16), g0, s0); s0 = fmaf(__uint_as_float(gx[h][d] & 0xffff0000u), g1, s0);
        s1 = fmaf(__uint_as_float(gx[2 + h][d] << 16), g0, s1); s1 = fmaf(__uint_as_float(gx[2 + h][d] & 0xffff0000u), g1, s1);
      }
    s0 += dpp<XOR1>(s0); s0 += dpp<XOR2>(s0); s0 += dpp<ROR4>(s0); s0 += dpp<ROR8>(s0); s0 = x16_sum(s0);
    s1 += dpp<XOR1>(s1); s1 += dpp<XOR2>(s1); s1 += dpp<ROR4>(s1); s1 += dpp<ROR8>(s1); s1 = x16_sum(s1);
    if (lane < 2) {
      const float z = lane ? s1 : s0;
      const float gt = 1.0f / (1.0f + __expf(-z));
      *(LAS float*)(L + L_GATE + lane * 4) = b ? 1.0f - gt : gt;
    }
  }
  STAMP(2);
  const int q_vo = WS_QP + ((kvh * 2 + b) * 2 + (tid >> 8)) * 16384 + ((tid & 255) >> 4) * 512 + (tid & 15) * 16;
  v4u q0 = ld16<SC1>(rws, q_vo, 0), q1 = ld16<SC1>(rws, q_vo + 256, 0), q2 = ld16<SC1>(rws, q_vo, 8192), q3 = ld16<SC1>(rws, q_vo + 256, 8192);
  SB();
#pragma unroll
  for (int j = 0; j < 4; ++j) avf[j] = ld16<KV_AUX>(rav, v_vo + j * 1024, a_so);
  v4u kf[8][4], vf[8][4];
#define ISSUE_B(n) do { if ((n) < 8) { _Pragma("unroll") for (int j_ = 0; j_ < 4; ++j_) kf[(n) & 7][j_] = ld16<KV_AUX>(rbk, k_vo + j_ * 64, b_so + ((n) & 7) * 32768); } \
    else if ((n) < 16) { _Pragma("unroll") for (int j_ = 0; j_ < 4; ++j_) vf[(n) & 7][j_] = ld16<KV_AUX>(rbv, v_vo + j_ * 1024, b_so + ((n) & 7) * 32768); } SB(); } while (0)
#pragma unroll
  for (int n = 0; n < PRE; ++n) ISSUE_B(n);
  {
    unsigned bad = (bad2(q0, tag) | bad2(q1, tag)) | (bad2(q2, tag) | bad2(q3, tag));
    unsigned spin = 0;
    for (; !__all(bad == 0) && spin < SPIN_CAP; ++spin) {
      MEMFENCE();
      q0 = ld16<SC1>(rws, q_vo, 0); q1 = ld16<SC1>(rws, q_vo + 256, 0); q2 = ld16<SC1>(rws, q_vo, 8192); q3 = ld16<SC1>(rws, q_vo + 256, 8192);
      bad = (bad2(q0, tag) | bad2(q1, tag)) | (bad2(q2, tag) | bad2(q3, tag));
    }
    COUNT(15, spin);
    const int pos = tid & 255, blk = pos >> 4, half = (pos >> 3) & 1, t = pos & 7;
    const int col = blk * 64 + half * 32 + t;
    LAS unsigned char* dq = L + L_Q + ((tid >> 8) * 8 + (col >> 7)) * 272 + (col & 127) * 2;
    const unsigned p01 = cvtpk(dec(q0.x) + dec(q2.x), dec(q0.z) + dec(q2.z)), p23 = cvtpk(dec(q1.x) + dec(q3.x), dec(q1.z) + dec(q3.z));
    *(LAS unsigned short*)(dq) = (unsigned short)p01;
    *(LAS unsigned short*)(dq + 16) = (unsigned short)(p01 >> 16);
    *(LAS unsigned short*)(dq + 32) = (unsigned short)p23;
    *(LAS unsigned short*)(dq + 48) = (unsigned short)(p23 >> 16);
  }
  STAMP(3);
  BAR();
  v4u qa[4], qb[4];
#pragma unroll
  for (int k = 0; k < 4; ++k) {
    qa[k] = *(LAS v4u*)(L + L_Q + (c16 & 7) * 272 + k * 64 + g * 16);
    qb[k] = *(LAS v4u*)(L + L_Q + (8 + (c16 & 7)) * 272 + k * 64 + g * 16);
  }
  {
    f32x4 sa = {0.f, 0.f, 0.f, 0.f};
#pragma unroll
    for (int k = 0; k < 4; ++k) sa = mfma32(akf[k], qa[k], sa);
    SB();
    ISSUE_B(PRE);
    float m = fmaxf(fmaxf(sa.x, sa.y), fmaxf(sa.z, sa.w));
    m = x16_max(m) * CS;
    const float p0 = ex2(fmaf(sa.x, CS, -m)), p1 = ex2(fmaf(sa.y, CS, -m)), p2 = ex2(fmaf(sa.z, CS, -m)), p3 = ex2(fmaf(sa.w, CS, -m));
    const float l = x16_sum((p0 + p1) + (p2 + p3));
    const v2u pf = {cvtpk(p0, p1), cvtpk(p2, p3)};
    LAS unsigned char* dst = L + L_MA + (w * 32 + g * 8 + (c16 & 7)) * 144;
#pragma unroll
    for (int t = 0; t < 8; ++t) {
      const f32x4 o = mfma16(col4(avf[0], avf[1], avf[2], avf[3], t), pf, f32x4{0.f, 0.f, 0.f, 0.f});
      if (c16 < 8) *(LAS f32x4*)(dst + t * 16) = o;
    }
    if (lane < 8) { *(LAS float*)(L + L_MLA + (lane * 8 + w) * 4) = m; *(LAS float*)(L + L_MLA + 256 + (lane * 8 + w) * 4) = l; }
    SB();
    ISSUE_B(PRE + 1);
  }
  BAR();
#define MERGE_PUBLISH(LM, LML, BR) do { if (w < 4) { \
    const int piece = tid >> 3, ch = tid & 7, head = piece & 7, qt = piece >> 3; \
    const f32x4 ma = *(LAS f32x4*)(L + (LML) + head * 32), mb = *(LAS f32x4*)(L + (LML) + head * 32 + 16); \
    const f32x4 la = *(LAS f32x4*)(L + (LML) + 256 + head * 32), lb = *(LAS f32x4*)(L + (LML) + 256 + head * 32 + 16); \
    f32x4 ov[8]; \
    _Pragma("unroll") for (int ww = 0; ww < 8; ++ww) ov[ww] = *(LAS f32x4*)(L + (LM) + (ww * 32 + piece) * 144 + ch * 16); \
    const float M = fmaxf(fmaxf(fmaxf(ma.x, ma.y), fmaxf(ma.z, ma.w)), fmaxf(fmaxf(mb.x, mb.y), fmaxf(mb.z, mb.w))); \
    float e[8] = {ex2(ma.x - M), ex2(ma.y - M), ex2(ma.z - M), ex2(ma.w - M), ex2(mb.x - M), ex2(mb.y - M), ex2(mb.z - M), ex2(mb.w - M)}; \
    const float Ls = ((e[0] * la.x + e[1] * la.y) + (e[2] * la.z + e[3] * la.w)) + ((e[4] * lb.x + e[5] * lb.y) + (e[6] * lb.z + e[7] * lb.w)); \
    const f32x4 o = ((e[0] * ov[0] + e[1] * ov[1]) + (e[2] * ov[2] + e[3] * ov[3])) + ((e[4] * ov[4] + e[5] * ov[5]) + (e[6] * ov[6] + e[7] * ov[7])); \
    const int ib = WS_INBOX + (kvh * 64 + (BR) * 32 + head * 4 + qt) * INBOX_B; \
    st16(rws, gran2(o.x, o.y, tag), ib + IB_O + i * 256 + ch * 16, 0); \
    st16(rws, gran2(o.z, o.w, tag), ib + IB_O + i * 256 + 128 + ch * 16, 0); \
    if (ch < 2) st16(rws, ch ? gran2(Ls, 0.f, tag) : gran2raw(__float_as_uint(M) >> 16, __float_as_uint(M) & 0xFFFFu, tag), ib + IB_ML + i * 32 + ch * 16, 0); \
  } } while (0)
  MERGE_PUBLISH(L_MA, L_MLA, 0);
  STAMP(4);
  f32x4 sb[8];
#pragma unroll
  for (int u = 0; u < 8; ++u) {
    f32x4 a = {0.f, 0.f, 0.f, 0.f};
#pragma unroll
    for (int k = 0; k < 4; ++k) a = mfma32(kf[u][k], qb[k], a);
    sb[u] = a;
    SB();
    ISSUE_B(PRE + 2 + u);
    if (LOCKSTEP) asm volatile("s_barrier" ::: "memory");
  }
  STAMP(5);
  float mB, lB;
  v4u pf[4];
  {
    float m = fmaxf(fmaxf(sb[0].x, sb[0].y), fmaxf(sb[0].z, sb[0].w));
#pragma unroll
    for (int u = 1; u < 8; ++u) m = fmaxf(m, fmaxf(fmaxf(sb[u].x, sb[u].y), fmaxf(sb[u].z, sb[u].w)));
    m = x16_max(m) * CS;
    float l = 0.f;
#pragma unroll
    for (int tt = 0; tt < 4; ++tt) {
      float pp[8];
#pragma unroll
      for (int j = 0; j < 4; ++j) { pp[j] = ex2(fmaf(sb[2 * tt][j], CS, -m)); pp[4 + j] = ex2(fmaf(sb[2 * tt + 1][j], CS, -m)); }
      l += ((pp[0] + pp[1]) + (pp[2] + pp[3])) + ((pp[4] + pp[5]) + (pp[6] + pp[7]));
      pf[tt] = v4u{cvtpk(pp[0], pp[1]), cvtpk(pp[2], pp[3]), cvtpk(pp[4], pp[5]), cvtpk(pp[6], pp[7])};
    }
    mB = m; lB = x16_sum(l);
  }
  f32x4 acc[8];
#pragma unroll
  for (int t = 0; t < 8; ++t) acc[t] = f32x4{0.f, 0.f, 0.f, 0.f};
#pragma unroll
  for (int tt = 0; tt < 4; ++tt) {
#pragma unroll
    for (int t = 0; t < 8; ++t)
      acc[t] = mfma32(col8(vf[2 * tt][0], vf[2 * tt][1], vf[2 * tt][2], vf[2 * tt][3], vf[2 * tt + 1][0], vf[2 * tt + 1][1], vf[2 * tt + 1][2], vf[2 * tt + 1][3], t), pf[tt], acc[t]);
    SB();
    ISSUE_B(PRE + 10 + 2 * tt);
    ISSUE_B(PRE + 11 + 2 * tt);
    if (LOCKSTEP && PRE + 10 + 2 * tt < 16) asm volatile("s_barrier" ::: "memory");
  }
  STAMP(6);
  {
    LAS unsigned char* dst = L + L_MB + (w * 32 + g * 8 + (c16 & 7)) * 144;
    if (c16 < 8) {
#pragma unroll
      for (int t = 0; t < 8; ++t) *(LAS f32x4*)(dst + t * 16) = acc[t];
    }
    if (lane < 8) { *(LAS float*)(L + L_MLB + (lane * 8 + w) * 4) = mB; *(LAS float*)(L + L_MLB + 256 + (lane * 8 + w) * 4) = lB; }
  }
  BAR();
  MERGE_PUBLISH(L_MB, L_MLB, 1);
  STAMP(7);
  v4u wof[8];
#pragma unroll
  for (int t = 0; t < 8; ++t) { wof[t] = col8(wo[0], wo[1], wo[2], wo[3], wo[4], wo[5], wo[6], wo[7], t); asm volatile("" : "+v"(wof[t])); }
  const float gate = *(LAS float*)(L + L_GATE + (c16 & 1) * 4);
  SB();
  {
    const int ib = WS_INBOX + idx * INBOX_B;
    const int ovo = (tid >> 3) * 256 + (tid & 7) * 16, mvo = (tid >> 3) * 32;
    v4u o0 = ld16<SC1>(rws, ovo, ib + IB_O), o1 = ld16<SC1>(rws, ovo + 128, ib + IB_O), m0 = ld16<SC1>(rws, mvo, ib + IB_ML), m1 = ld16<SC1>(rws, mvo + 16, ib + IB_ML);
    unsigned bad = (bad2(o0, tag) | bad2(o1, tag)) | (bad2(m0, tag) | bad2(m1, tag));
    unsigned spin = 0;
    for (; !__all(bad == 0) && spin < SPIN_CAP; ++spin) {
      MEMFENCE();
      o0 = ld16<SC1>(rws, ovo, ib + IB_O); o1 = ld16<SC1>(rws, ovo + 128, ib + IB_O); m0 = ld16<SC1>(rws, mvo, ib + IB_ML); m1 = ld16<SC1>(rws, mvo + 16, ib + IB_ML);
      bad = (bad2(o0, tag) | bad2(o1, tag)) | (bad2(m0, tag) | bad2(m1, tag));
    }
    STAMP(8);
    const float m = __uint_as_float((m0.x << 16) | (m0.z & 0xFFFFu));
    float M = fmaxf(m, dpp<ROR8>(m)); M = x16_max(M);
    const float e = ex2(m - M);
    f32x4 o = {e * dec(o0.x), e * dec(o0.z), e * dec(o1.x), e * dec(o1.z)};
    float l = e * dec(m1.x);
    o.x += dpp<ROR8>(o.x); o.y += dpp<ROR8>(o.y); o.z += dpp<ROR8>(o.z); o.w += dpp<ROR8>(o.w); l += dpp<ROR8>(l);
    o.x = x16_sum(o.x); o.y = x16_sum(o.y); o.z = x16_sum(o.z); o.w = x16_sum(o.w); l = x16_sum(l);
    if (lane < 8) *(LAS f32x4*)(L + L_T2 + w * 128 + lane * 16) = o;
    if (lane == 0) { *(LAS float*)(L + L_T2ML + w * 4) = M; *(LAS float*)(L + L_T2ML + 32 + w * 4) = l; }
  }
  BAR();
  {
    const int bb = c16 & 1;
    const f32x4 Mw = *(LAS f32x4*)(L + L_T2ML + bb * 16), Lw = *(LAS f32x4*)(L + L_T2ML + 32 + bb * 16);
    LAS unsigned char* src = L + L_T2 + bb * 512 + g * 32;
    const f32x4 a0 = *(LAS f32x4*)(src), a1 = *(LAS f32x4*)(src + 128), a2 = *(LAS f32x4*)(src + 256), a3 = *(LAS f32x4*)(src + 384);
    const f32x4 b0 = *(LAS f32x4*)(src + 16), b1 = *(LAS f32x4*)(src + 144), b2 = *(LAS f32x4*)(src + 272), b3 = *(LAS f32x4*)(src + 400);
    const float M = fmaxf(fmaxf(Mw.x, Mw.y), fmaxf(Mw.z, Mw.w));
    float e0 = ex2(Mw.x - M), e1 = ex2(Mw.y - M), e2 = ex2(Mw.z - M), e3 = ex2(Mw.w - M);
    const float coef = gate * __builtin_amdgcn_rcpf((e0 * Lw.x + e1 * Lw.y) + (e2 * Lw.z + e3 * Lw.w));
    e0 *= coef; e1 *= coef; e2 *= coef; e3 *= coef;
    const f32x4 oa = (e0 * a0 + e1 * a1) + (e2 * a2 + e3 * a3);
    const f32x4 ob = (e0 * b0 + e1 * b1) + (e2 * b2 + e3 * b3);
    const v4u of = {cvtpk(oa.x, oa.y), cvtpk(oa.z, oa.w), cvtpk(ob.x, ob.y), cvtpk(ob.z, ob.w)};
    f32x4 d[8];
#pragma unroll
    for (int t = 0; t < 8; ++t) d[t] = mfma32(of, wof[t], f32x4{0.f, 0.f, 0.f, 0.f});
    f32x4 v;
#pragma unroll
    for (int k = 0; k < 4; ++k) { const float lo = (g & 2) ? d[k][1] : d[k][0], hi = (g & 2) ? d[4 + k][1] : d[4 + k][0]; v[k] = (g & 1) ? hi : lo; }
    const int to = WS_INBOX + ((g >> 1) * 128 + 16 * w + c16) * INBOX_B + IB_T3 + idx * 64 + (g & 1) * 32;
    st16(rws, gran2(v.x, v.y, tag), to, 0);
    st16(rws, gran2(v.z, v.w, tag), to + 16, 0);
  }
  STAMP(9);
  {
    const int ib = WS_INBOX + idx * INBOX_B + IB_T3;
    v4u v0 = ld16<SC1>(rws, tid * 32, ib), v1 = ld16<SC1>(rws, tid * 32 + 16, ib);
    unsigned bad = bad2(v0, tag) | bad2(v1, tag);
    unsigned spin = 0;
    for (; !__all(bad == 0) && spin < SPIN_CAP; ++spin) { MEMFENCE(); v0 = ld16<SC1>(rws, tid * 32, ib); v1 = ld16<SC1>(rws, tid * 32 + 16, ib); bad = bad2(v0, tag) | bad2(v1, tag); }
    STAMP(10);
    COUNT(14, spin);
    f32x4 v = {dec(v0.x), dec(v0.z), dec(v1.x), dec(v1.z)};
#pragma unroll
    for (int k = 0; k < 4; ++k) { float z = v[k]; z += dpp<XOR2>(z); z += dpp<ROR4>(z); z += dpp<ROR8>(z); v[k] = x16_sum(z); }
    if (lane < 2) *(LAS f32x4*)(L + L_T3 + w * 32 + lane * 16) = v;
  }
  BAR();
  if (tid < 4) {
    float a0 = 0.f, a1 = 0.f;
#pragma unroll
    for (int ww = 0; ww < 8; ++ww) { a0 += *(LAS float*)(L + L_T3 + ww * 32 + tid * 8); a1 += *(LAS float*)(L + L_T3 + ww * 32 + tid * 8 + 4); }
    ((unsigned*)p.out)[idx * 4 + tid] = cvtpk(a0, a1);
    if (idx == 0 && tid == 0) __builtin_amdgcn_raw_buffer_store_b64(v2u{tag.lo, tag.hi}, rws, WS_EPOCH, 0, SC1);
  }
}
}

extern "C" void kernel_launch(void* const* d_in, const int* in_sizes, int n_in, void* d_out, int out_size, void* d_ws, size_t ws_size,
                              hipStream_t stream) {
  Params p;
  p.x = d_in[0]; p.ak = d_in[1]; p.av = d_in[2]; p.wqa = d_in[3]; p.woa = d_in[4];
  p.bk = d_in[6]; p.bv = d_in[7]; p.wqb = d_in[8]; p.wob = d_in[9]; p.gw = d_in[10];
  p.out = d_out; p.ws = d_ws;
  hipLaunchKernelGGL(mix_decode, dim3(NWG), dim3(NTHR), 0, stream, p);
}
```

```cpp
#define PAD_US 80
#include <hip/hip_runtime.h>
#include <hip/hip_bf16.h>

#define KV_AUX 2
#define W_AUX 2
#define PRE 2
#define LOCKSTEP 1
#define STAMPS 0

namespace {
typedef unsigned v4u __attribute__((ext_vector_type(4)));
typedef unsigned v2u __attribute__((ext_vector_type(2)));
typedef float f32x4 __attribute__((ext_vector_type(4)));
typedef short bf16x8 __attribute__((ext_vector_type(8)));
typedef short bf16x4 __attribute__((ext_vector_type(4)));
typedef unsigned long long u64;
#define LAS __attribute__((address_space(3)))
typedef __amdgpu_buffer_rsrc_t rsrc_t;

constexpr int NWG = 256, NTHR = 512;
constexpr int SK = 32768, WIN = 4096;
constexpr int SC1 = 16;
constexpr int RW3 = 0x00020000;
constexpr int WS_EPOCH = 0;
constexpr int WS_QP = 4096;
constexpr int WS_INBOX = WS_QP + 262144;
constexpr int IB_O = 0, IB_ML = 16384, IB_T3 = 18432, INBOX_B = 34816;
constexpr int WS_END = WS_INBOX + NWG * INBOX_B;
constexpr int WS_STAMP = WS_END;
constexpr int WS_BYTES = WS_STAMP + NWG * 8 * 16 * 8;
constexpr int L_XQ = 0;
constexpr int L_Q = L_XQ + 64 * 144;
constexpr int L_MA = L_Q + 16 * 272;
constexpr int L_MB = L_MA + 256 * 144;
constexpr int L_MLA = L_MB + 256 * 144;
constexpr int L_MLB = L_MLA + 512;
constexpr int L_T2 = L_MLB + 512;
constexpr int L_T2ML = L_T2 + 1024;
constexpr int L_GATE = L_T2ML + 64;
constexpr int L_T3 = L_GATE + 16;
constexpr int L_STAMP = L_T3 + 256;
constexpr int L_END = L_STAMP + (STAMPS ? 1024 : 0);
constexpr float CS = 0.08838834764831845f * 1.4426950408889634f;
constexpr unsigned SPIN_CAP = 1u << 16;

struct Params {
  const void* x; const void* ak; const void* av; const void* wqa; const void* woa;
  const void* bk; const void* bv; const void* wqb; const void* wob; const void* gw;
  void* out; void* ws;
};

__device__ __forceinline__ rsrc_t mk(const void* p, unsigned bytes) { return __builtin_amdgcn_make_buffer_rsrc((void*)p, 0, (int)bytes, RW3); }
template <int AUX> __device__ __forceinline__ v4u ld16(rsrc_t r, int voff, int soff) { return __builtin_amdgcn_raw_buffer_load_b128(r, voff, soff, AUX); }
__device__ __forceinline__ void st16(rsrc_t r, v4u v, int voff, int soff) { __builtin_amdgcn_raw_buffer_store_b128(v, r, voff, soff, SC1); }
__device__ __forceinline__ f32x4 mfma32(v4u a, v4u b, f32x4 c) {
  return __builtin_amdgcn_mfma_f32_16x16x32_bf16(__builtin_bit_cast(bf16x8, a), __builtin_bit_cast(bf16x8, b), c, 0, 0, 0);
}
__device__ __forceinline__ f32x4 mfma16(v2u a, v2u b, f32x4 c) {
  return __builtin_amdgcn_mfma_f32_16x16x16bf16_1k(__builtin_bit_cast(bf16x4, a), __builtin_bit_cast(bf16x4, b), c, 0, 0, 0);
}
__device__ __forceinline__ unsigned permlo(unsigned hi, unsigned lo) { return __builtin_amdgcn_perm(hi, lo, 0x05040100u); }
__device__ __forceinline__ unsigned permhi(unsigned hi, unsigned lo) { return __builtin_amdgcn_perm(hi, lo, 0x07060302u); }
__device__ __forceinline__ v4u col8(const v4u& r0, const v4u& r1, const v4u& r2, const v4u& r3, const v4u& r4, const v4u& r5, const v4u& r6, const v4u& r7, int t) {
  const int d = t >> 1;
  v4u o;
  if (t & 1) { o.x = permhi(r1[d], r0[d]); o.y = permhi(r3[d], r2[d]); o.z = permhi(r5[d], r4[d]); o.w = permhi(r7[d], r6[d]); }
  else       { o.x = permlo(r1[d], r0[d]); o.y = permlo(r3[d], r2[d]); o.z = permlo(r5[d], r4[d]); o.w = permlo(r7[d], r6[d]); }
  return o;
}
__device__ __forceinline__ v2u col4(const v4u& r0, const v4u& r1, const v4u& r2, const v4u& r3, int t) {
  const int d = t >> 1;
  v2u o;
  if (t & 1) { o.x = permhi(r1[d], r0[d]); o.y = permhi(r3[d], r2[d]); }
  else       { o.x = permlo(r1[d], r0[d]); o.y = permlo(r3[d], r2[d]); }
  return o;
}
__device__ __forceinline__ unsigned cvtpk(float lo, float hi) {
  typedef __bf16 b2 __attribute__((ext_vector_type(2)));
  typedef float f2 __attribute__((ext_vector_type(2)));
  f2 f = {lo, hi};
  return __builtin_bit_cast(unsigned, __builtin_convertvector(f, b2));
}
template <int CTRL> __device__ __forceinline__ float dpp(float x) {
  return __builtin_bit_cast(float, __builtin_amdgcn_mov_dpp(__builtin_bit_cast(int, x), CTRL, 0xf, 0xf, true));
}
constexpr int XOR1 = 0xB1, XOR2 = 0x4E, ROR4 = 0x124, ROR8 = 0x128;
__device__ __forceinline__ float x16_max(float x) {
  auto s = __builtin_amdgcn_permlane16_swap(__float_as_uint(x), __float_as_uint(x), false, false);
  x = fmaxf(__uint_as_float(s[0]), __uint_as_float(s[1]));
  auto t = __builtin_amdgcn_permlane32_swap(__float_as_uint(x), __float_as_uint(x), false, false);
  return fmaxf(__uint_as_float(t[0]), __uint_as_float(t[1]));
}
__device__ __forceinline__ float x32_sum(float x) {
  auto t = __builtin_amdgcn_permlane32_swap(__float_as_uint(x), __float_as_uint(x), false, false);
  return __uint_as_float(t[0]) + __uint_as_float(t[1]);
}
__device__ __forceinline__ float x16_sum(float x) {
  auto s = __builtin_amdgcn_permlane16_swap(__float_as_uint(x), __float_as_uint(x), false, false);
  return x32_sum(__uint_as_float(s[0]) + __uint_as_float(s[1]));
}
__device__ __forceinline__ float ex2(float x) { return __builtin_amdgcn_exp2f(x); }
struct Tag { unsigned lo, hi; };
__device__ __forceinline__ unsigned enc(float f, Tag t) { return ((__float_as_uint(f) + 0x800u) >> 12) | t.lo; }
__device__ __forceinline__ v4u gran2(float a, float b, Tag t) { return v4u{enc(a, t), t.hi, enc(b, t), t.hi}; }
__device__ __forceinline__ v4u gran2raw(unsigned a, unsigned b, Tag t) { return v4u{a | t.lo, t.hi, b | t.lo, t.hi}; }
__device__ __forceinline__ unsigned bad2(v4u v, Tag t) { return ((v.y ^ t.hi) | (v.w ^ t.hi)) | (((v.x ^ t.lo) | (v.z ^ t.lo)) >> 20); }
__device__ __forceinline__ float dec(unsigned lo) { return __uint_as_float(lo << 12); }
#define BAR() asm volatile("s_waitcnt lgkmcnt(0)\n\ts_barrier" ::: "memory")
#define SB() __builtin_amdgcn_sched_barrier(0)
#define MEMFENCE() asm volatile("" ::: "memory")
#define STAMP(i) do {} while (0)
#define COUNT(i, v) do {} while (0)

__global__ void __launch_bounds__(NTHR, 2) mix_decode(Params p) {
  __shared__ __attribute__((aligned(16))) unsigned char smem[L_END];
  LAS unsigned char* L = (LAS unsigned char*)smem;
  const int tid = threadIdx.x, lane = tid & 63, w = __builtin_amdgcn_readfirstlane(tid >> 6);
  const int g = lane >> 4, c16 = lane & 15;
  const int c = blockIdx.x, grp = c & 7, kvh = grp >> 1, b = grp & 1, s = c >> 3;
  const int i = b * 32 + s, idx = kvh * 64 + i;
  STAMP(0);
  const u64 t_begin = __builtin_amdgcn_s_memrealtime();
  const rsrc_t rws = mk(p.ws, WS_BYTES);
  const rsrc_t rx = mk(p.x, 4096);
  const v2u ep = __builtin_amdgcn_raw_buffer_load_b64(rws, WS_EPOCH, 0, SC1);
  const int cg = s >> 1, kh = s & 1;
  const v4u xf = ld16<0>(rx, (c16 & 1) * 2048 + (kh * 512 + 64 * w + 32 * ((c16 >> 1) & 1) + 8 * g) * 2, 0);
  v4u wq[8], wo[8];
  {
    const rsrc_t rq = mk(b ? p.wqb : p.wqa, 1024 * 4096 * 2);
    const int vo = (32 * (c16 >> 3) + 8 * g) * 8192 + (c16 & 7) * 16;
    const int so = (kh * 512 + 64 * w) * 8192 + (kvh * 1024 + cg * 64) * 2;
#pragma unroll
    for (int j = 0; j < 8; ++j) wq[j] = ld16<W_AUX>(rq, vo, so + j * 8192);
  }
  SB();
  BAR();
  const int hc = (s >> 2) & 7, qtc = s & 3;
  const rsrc_t ro = mk(b ? p.wob : p.woa, 4096 * 1024 * 2);
  const int wo_vo = 2 * g * 2048 + c16 * 16;
  const int wo_so = ((kvh * 8 + hc) * 128 + qtc * 32) * 2048 + w * 256;
#pragma unroll
  for (int j = 0; j < 4; ++j) wo[j] = ld16<W_AUX>(ro, wo_vo, wo_so + (8 * (j & 3) + (j >> 2)) * 2048);
  SB();
  Tag tag;
  { const u64 e = (((u64)ep.y << 32) | ep.x) + (1ull << 20); tag.lo = (unsigned)e & 0xFFF00000u; tag.hi = (unsigned)(e >> 32); }
  {
    LAS unsigned char* dst = L + L_XQ + (((w * 2 + (g >> 1)) * 2 + (c16 & 1)) * 2 + (g & 1)) * 144;
    const bool wr = c16 < 4 && ((c16 >> 1) == (g >> 1));
#pragma unroll
    for (int t = 0; t < 8; ++t) {
      const v4u a = col8(wq[0], wq[1], wq[2], wq[3], wq[4], wq[5], wq[6], wq[7], t);
      const f32x4 d = mfma32(a, xf, f32x4{0.f, 0.f, 0.f, 0.f});
      if (wr) *(LAS f32x4*)(dst + t * 16) = d;
    }
  }
  STAMP(1);
  BAR();
  STAMP(12);
  if (w == 0) {
    const int o32 = lane & 31, ph = lane >> 5;
    const int bb = o32 >> 4, half = (o32 >> 3) & 1, t = o32 & 7;
    LAS unsigned char* src = L + L_XQ + ((ph * 8 * 2 + bb) * 2 + half) * 144 + t * 16;
    f32x4 r[8];
#pragma unroll
    for (int k = 0; k < 8; ++k) r[k] = *(LAS f32x4*)(src + k * 576);
    SB();
    f32x4 a = ((r[0] + r[1]) + (r[2] + r[3])) + ((r[4] + r[5]) + (r[6] + r[7]));
#pragma unroll
    for (int k = 0; k < 4; ++k) a[k] = x32_sum(a[k]);
    const int o2 = WS_QP + ((((kvh * 2 + bb) * 2 + b) * 2 + kh) * 16 + cg) * 512 + ph * 256 + (half * 8 + t) * 16;
    st16(rws, gran2(ph ? a.z : a.x, ph ? a.w : a.y, tag), o2, 0);
  }
  SB();
  STAMP(13);
#pragma unroll
  for (int j = 4; j < 8; ++j) wo[j] = ld16<W_AUX>(ro, wo_vo, wo_so + (8 * (j & 3) + (j >> 2)) * 2048);
  SB();
  v4u gx[6];
  if (w == 7) {
    const rsrc_t rg = mk(p.gw, 2048);
    gx[0] = ld16<0>(rx, lane * 16, 0); gx[1] = ld16<0>(rx, lane * 16, 1024);
    gx[2] = ld16<0>(rx, lane * 16, 2048); gx[3] = ld16<0>(rx, lane * 16, 3072);
    gx[4] = ld16<0>(rg, lane * 16, 0); gx[5] = ld16<0>(rg, lane * 16, 1024);
  }
  SB();
  const unsigned gbase = (unsigned)(b * 4 + kvh) * (SK * 256);
  const rsrc_t rak = mk(p.ak, 2u * 4 * SK * 256), rav = mk(p.av, 2u * 4 * SK * 256);
  const rsrc_t rbk = mk(p.bk, 2u * 4 * SK * 256), rbv = mk(p.bv, 2u * 4 * SK * 256);
  const int k_vo = (4 * (c16 & 3) + (c16 >> 2)) * 256 + g * 16;
  const int v_vo = lane * 16;
  const int a_so = gbase + (SK - WIN + s * 128 + w * 16) * 256;
  const int b_so = gbase + (s * 1024 + w * 16) * 256;
  v4u akf[4], avf[4];
#pragma unroll
  for (int j = 0; j < 4; ++j) akf[j] = ld16<KV_AUX>(rak, k_vo + j * 64, a_so);
  SB();
  if (w == 7) {
    float s0 = 0.f, s1 = 0.f;
#pragma unroll
    for (int h = 0; h < 2; ++h)
#pragma unroll
      for (int d = 0; d < 4; ++d) {
        const float g0 = __uint_as_float(gx[4 + h][d] << 16), g1 = __uint_as_float(gx[4 + h][d] & 0xffff0000u);
        s0 = fmaf(__uint_as_float(gx[h][d] << 16), g0, s0); s0 = fmaf(__uint_as_float(gx[h][d] & 0xffff0000u), g1, s0);
        s1 = fmaf(__uint_as_float(gx[2 + h][d] << 16), g0, s1); s1 = fmaf(__uint_as_float(gx[2 + h][d] & 0xffff0000u), g1, s1);
      }
    s0 += dpp<XOR1>(s0); s0 += dpp<XOR2>(s0); s0 += dpp<ROR4>(s0); s0 += dpp<ROR8>(s0); s0 = x16_sum(s0);
    s1 += dpp<XOR1>(s1); s1 += dpp<XOR2>(s1); s1 += dpp<ROR4>(s1); s1 += dpp<ROR8>(s1); s1 = x16_sum(s1);
    if (lane < 2) {
      const float z = lane ? s1 : s0;
      const float gt = 1.0f / (1.0f + __expf(-z));
      *(LAS float*)(L + L_GATE + lane * 4) = b ? 1.0f - gt : gt;
    }
  }
  STAMP(2);
  const int q_vo = WS_QP + ((kvh * 2 + b) * 2 + (tid >> 8)) * 16384 + ((tid & 255) >> 4) * 512 + (tid & 15) * 16;
  v4u q0 = ld16<SC1>(rws, q_vo, 0), q1 = ld16<SC1>(rws, q_vo + 256, 0), q2 = ld16<SC1>(rws, q_vo, 8192), q3 = ld16<SC1>(rws, q_vo + 256, 8192);
  SB();
#pragma unroll
  for (int j = 0; j < 4; ++j) avf[j] = ld16<KV_AUX>(rav, v_vo + j * 1024, a_so);
  v4u kf[8][4], vf[8][4];
#define ISSUE_B(n) do { if ((n) < 8) { _Pragma("unroll") for (int j_ = 0; j_ < 4; ++j_) kf[(n) & 7][j_] = ld16<KV_AUX>(rbk, k_vo + j_ * 64, b_so + ((n) & 7) * 32768); } \
    else if ((n) < 16) { _Pragma("unroll") for (int j_ = 0; j_ < 4; ++j_) vf[(n) & 7][j_] = ld16<KV_AUX>(rbv, v_vo + j_ * 1024, b_so + ((n) & 7) * 32768); } SB(); } while (0)
#pragma unroll
  for (int n = 0; n < PRE; ++n) ISSUE_B(n);
  {
    unsigned bad = (bad2(q0, tag) | bad2(q1, tag)) | (bad2(q2, tag) | bad2(q3, tag));
    unsigned spin = 0;
    for (; !__all(bad == 0) && spin < SPIN_CAP; ++spin) {
      MEMFENCE();
      q0 = ld16<SC1>(rws, q_vo, 0); q1 = ld16<SC1>(rws, q_vo + 256, 0); q2 = ld16<SC1>(rws, q_vo, 8192); q3 = ld16<SC1>(rws, q_vo + 256, 8192);
      bad = (bad2(q0, tag) | bad2(q1, tag)) | (bad2(q2, tag) | bad2(q3, tag));
    }
    COUNT(15, spin);
    const int pos = tid & 255, blk = pos >> 4, half = (pos >> 3) & 1, t = pos & 7;
    const int col = blk * 64 + half * 32 + t;
    LAS unsigned char* dq = L + L_Q + ((tid >> 8) * 8 + (col >> 7)) * 272 + (col & 127) * 2;
    const unsigned p01 = cvtpk(dec(q0.x) + dec(q2.x), dec(q0.z) + dec(q2.z)), p23 = cvtpk(dec(q1.x) + dec(q3.x), dec(q1.z) + dec(q3.z));
    *(LAS unsigned short*)(dq) = (unsigned short)p01;
    *(LAS unsigned short*)(dq + 16) = (unsigned short)(p01 >> 16);
    *(LAS unsigned short*)(dq + 32) = (unsigned short)p23;
    *(LAS unsigned short*)(dq + 48) = (unsigned short)(p23 >> 16);
  }
  STAMP(3);
  BAR();
  v4u qa[4], qb[4];
#pragma unroll
  for (int k = 0; k < 4; ++k) {
    qa[k] = *(LAS v4u*)(L + L_Q + (c16 & 7) * 272 + k * 64 + g * 16);
    qb[k] = *(LAS v4u*)(L + L_Q + (8 + (c16 & 7)) * 272 + k * 64 + g * 16);
  }
  {
    f32x4 sa = {0.f, 0.f, 0.f, 0.f};
#pragma unroll
    for (int k = 0; k < 4; ++k) sa = mfma32(akf[k], qa[k], sa);
    SB();
    ISSUE_B(PRE);
    float m = fmaxf(fmaxf(sa.x, sa.y), fmaxf(sa.z, sa.w));
    m = x16_max(m) * CS;
    const float p0 = ex2(fmaf(sa.x, CS, -m)), p1 = ex2(fmaf(sa.y, CS, -m)), p2 = ex2(fmaf(sa.z, CS, -m)), p3 = ex2(fmaf(sa.w, CS, -m));
    const float l = x16_sum((p0 + p1) + (p2 + p3));
    const v2u pf = {cvtpk(p0, p1), cvtpk(p2, p3)};
    LAS unsigned char* dst = L + L_MA + (w * 32 + g * 8 + (c16 & 7)) * 144;
#pragma unroll
    for (int t = 0; t < 8; ++t) {
      const f32x4 o = mfma16(col4(avf[0], avf[1], avf[2], avf[3], t), pf, f32x4{0.f, 0.f, 0.f, 0.f});
      if (c16 < 8) *(LAS f32x4*)(dst + t * 16) = o;
    }
    if (lane < 8) { *(LAS float*)(L + L_MLA + (lane * 8 + w) * 4) = m; *(LAS float*)(L + L_MLA + 256 + (lane * 8 + w) * 4) = l; }
    SB();
    ISSUE_B(PRE + 1);
  }
  BAR();
#define MERGE_PUBLISH(LM, LML, BR) do { if (w < 4) { \
    const int piece = tid >> 3, ch = tid & 7, head = piece & 7, qt = piece >> 3; \
    const f32x4 ma = *(LAS f32x4*)(L + (LML) + head * 32), mb = *(LAS f32x4*)(L + (LML) + head * 32 + 16); \
    const f32x4 la = *(LAS f32x4*)(L + (LML) + 256 + head * 32), lb = *(LAS f32x4*)(L + (LML) + 256 + head * 32 + 16); \
    f32x4 ov[8]; \
    _Pragma("unroll") for (int ww = 0; ww < 8; ++ww) ov[ww] = *(LAS f32x4*)(L + (LM) + (ww * 32 + piece) * 144 + ch * 16); \
    const float M = fmaxf(fmaxf(fmaxf(ma.x, ma.y), fmaxf(ma.z, ma.w)), fmaxf(fmaxf(mb.x, mb.y), fmaxf(mb.z, mb.w))); \
    float e[8] = {ex2(ma.x - M), ex2(ma.y - M), ex2(ma.z - M), ex2(ma.w - M), ex2(mb.x - M), ex2(mb.y - M), ex2(mb.z - M), ex2(mb.w - M)}; \
    const float Ls = ((e[0] * la.x + e[1] * la.y) + (e[2] * la.z + e[3] * la.w)) + ((e[4] * lb.x + e[5] * lb.y) + (e[6] * lb.z + e[7] * lb.w)); \
    const f32x4 o = ((e[0] * ov[0] + e[1] * ov[1]) + (e[2] * ov[2] + e[3] * ov[3])) + ((e[4] * ov[4] + e[5] * ov[5]) + (e[6] * ov[6] + e[7] * ov[7])); \
    const int ib = WS_INBOX + (kvh * 64 + (BR) * 32 + head * 4 + qt) * INBOX_B; \
    st16(rws, gran2(o.x, o.y, tag), ib + IB_O + i * 256 + ch * 16, 0); \
    st16(rws, gran2(o.z, o.w, tag), ib + IB_O + i * 256 + 128 + ch * 16, 0); \
    if (ch < 2) st16(rws, ch ? gran2(Ls, 0.f, tag) : gran2raw(__float_as_uint(M) >> 16, __float_as_uint(M) & 0xFFFFu, tag), ib + IB_ML + i * 32 + ch * 16, 0); \
  } } while (0)
  MERGE_PUBLISH(L_MA, L_MLA, 0);
  STAMP(4);
  f32x4 sb[8];
#pragma unroll
  for (int u = 0; u < 8; ++u) {
    f32x4 a = {0.f, 0.f, 0.f, 0.f};
#pragma unroll
    for (int k = 0; k < 4; ++k) a = mfma32(kf[u][k], qb[k], a);
    sb[u] = a;
    SB();
    ISSUE_B(PRE + 2 + u);
    if (LOCKSTEP) asm volatile("s_barrier" ::: "memory");
  }
  STAMP(5);
  float mB, lB;
  v4u pf[4];
  {
    float m = fmaxf(fmaxf(sb[0].x, sb[0].y), fmaxf(sb[0].z, sb[0].w));
#pragma unroll
    for (int u = 1; u < 8; ++u) m = fmaxf(m, fmaxf(fmaxf(sb[u].x, sb[u].y), fmaxf(sb[u].z, sb[u].w)));
    m = x16_max(m) * CS;
    float l = 0.f;
#pragma unroll
    for (int tt = 0; tt < 4; ++tt) {
      float pp[8];
#pragma unroll
      for (int j = 0; j < 4; ++j) { pp[j] = ex2(fmaf(sb[2 * tt][j], CS, -m)); pp[4 + j] = ex2(fmaf(sb[2 * tt + 1][j], CS, -m)); }
      l += ((pp[0] + pp[1]) + (pp[2] + pp[3])) + ((pp[4] + pp[5]) + (pp[6] + pp[7]));
      pf[tt] = v4u{cvtpk(pp[0], pp[1]), cvtpk(pp[2], pp[3]), cvtpk(pp[4], pp[5]), cvtpk(pp[6], pp[7])};
    }
    mB = m; lB = x16_sum(l);
  }
  f32x4 acc[8];
#pragma unroll
  for (int t = 0; t < 8; ++t) acc[t] = f32x4{0.f, 0.f, 0.f, 0.f};
#pragma unroll
  for (int tt = 0; tt < 4; ++tt) {
#pragma unroll
    for (int t = 0; t < 8; ++t)
      acc[t] = mfma32(col8(vf[2 * tt][0], vf[2 * tt][1], vf[2 * tt][2], vf[2 * tt][3], vf[2 * tt + 1][0], vf[2 * tt + 1][1], vf[2 * tt + 1][2], vf[2 * tt + 1][3], t), pf[tt], acc[t]);
    SB();
    ISSUE_B(PRE + 10 + 2 * tt);
    ISSUE_B(PRE + 11 + 2 * tt);
    if (LOCKSTEP && PRE + 10 + 2 * tt < 16) asm volatile("s_barrier" ::: "memory");
  }
  STAMP(6);
  {
    LAS unsigned char* dst = L + L_MB + (w * 32 + g * 8 + (c16 & 7)) * 144;
    if (c16 < 8) {
#pragma unroll
      for (int t = 0; t < 8; ++t) *(LAS f32x4*)(dst + t * 16) = acc[t];
    }
    if (lane < 8) { *(LAS float*)(L + L_MLB + (lane * 8 + w) * 4) = mB; *(LAS float*)(L + L_MLB + 256 + (lane * 8 + w) * 4) = lB; }
  }
  BAR();
  MERGE_PUBLISH(L_MB, L_MLB, 1);
  STAMP(7);
  v4u wof[8];
#pragma unroll
  for (int t = 0; t < 8; ++t) { wof[t] = col8(wo[0], wo[1], wo[2], wo[3], wo[4], wo[5], wo[6], wo[7], t); asm volatile("" : "+v"(wof[t])); }
  const float gate = *(LAS float*)(L + L_GATE + (c16 & 1) * 4);
  SB();
  {
    const int ib = WS_INBOX + idx * INBOX_B;
    const int ovo = (tid >> 3) * 256 + (tid & 7) * 16, mvo = (tid >> 3) * 32;
    v4u o0 = ld16<SC1>(rws, ovo, ib + IB_O), o1 = ld16<SC1>(rws, ovo + 128, ib + IB_O), m0 = ld16<SC1>(rws, mvo, ib + IB_ML), m1 = ld16<SC1>(rws, mvo + 16, ib + IB_ML);
    unsigned bad = (bad2(o0, tag) | bad2(o1, tag)) | (bad2(m0, tag) | bad2(m1, tag));
    unsigned spin = 0;
    for (; !__all(bad == 0) && spin < SPIN_CAP; ++spin) {
      MEMFENCE();
      o0 = ld16<SC1>(rws, ovo, ib + IB_O); o1 = ld16<SC1>(rws, ovo + 128, ib + IB_O); m0 = ld16<SC1>(rws, mvo, ib + IB_ML); m1 = ld16<SC1>(rws, mvo + 16, ib + IB_ML);
      bad = (bad2(o0, tag) | bad2(o1, tag)) | (bad2(m0, tag) | bad2(m1, tag));
    }
    STAMP(8);
    const float m = __uint_as_float((m0.x << 16) | (m0.z & 0xFFFFu));
    float M = fmaxf(m, dpp<ROR8>(m)); M = x16_max(M);
    const float e = ex2(m - M);
    f32x4 o = {e * dec(o0.x), e * dec(o0.z), e * dec(o1.x), e * dec(o1.z)};
    float l = e * dec(m1.x);
    o.x += dpp<ROR8>(o.x); o.y += dpp<ROR8>(o.y); o.z += dpp<ROR8>(o.z); o.w += dpp<ROR8>(o.w); l += dpp<ROR8>(l);
    o.x = x16_sum(o.x); o.y = x16_sum(o.y); o.z = x16_sum(o.z); o.w = x16_sum(o.w); l = x16_sum(l);
    if (lane < 8) *(LAS f32x4*)(L + L_T2 + w * 128 + lane * 16) = o;
    if (lane == 0) { *(LAS float*)(L + L_T2ML + w * 4) = M; *(LAS float*)(L + L_T2ML + 32 + w * 4) = l; }
  }
  BAR();
  {
    const int bb = c16 & 1;
    const f32x4 Mw = *(LAS f32x4*)(L + L_T2ML + bb * 16), Lw = *(LAS f32x4*)(L + L_T2ML + 32 + bb * 16);
    LAS unsigned char* src = L + L_T2 + bb * 512 + g * 32;
    const f32x4 a0 = *(LAS f32x4*)(src), a1 = *(LAS f32x4*)(src + 128), a2 = *(LAS f32x4*)(src + 256), a3 = *(LAS f32x4*)(src + 384);
    const f32x4 b0 = *(LAS f32x4*)(src + 16), b1 = *(LAS f32x4*)(src + 144), b2 = *(LAS f32x4*)(src + 272), b3 = *(LAS f32x4*)(src + 400);
    const float M = fmaxf(fmaxf(Mw.x, Mw.y), fmaxf(Mw.z, Mw.w));
    float e0 = ex2(Mw.x - M), e1 = ex2(Mw.y - M), e2 = ex2(Mw.z - M), e3 = ex2(Mw.w - M);
    const float coef = gate * __builtin_amdgcn_rcpf((e0 * Lw.x + e1 * Lw.y) + (e2 * Lw.z + e3 * Lw.w));
    e0 *= coef; e1 *= coef; e2 *= coef; e3 *= coef;
    const f32x4 oa = (e0 * a0 + e1 * a1) + (e2 * a2 + e3 * a3);
    const f32x4 ob = (e0 * b0 + e1 * b1) + (e2 * b2 + e3 * b3);
    const v4u of = {cvtpk(oa.x, oa.y), cvtpk(oa.z, oa.w), cvtpk(ob.x, ob.y), cvtpk(ob.z, ob.w)};
    f32x4 d[8];
#pragma unroll
    for (int t = 0; t < 8; ++t) d[t] = mfma32(of, wof[t], f32x4{0.f, 0.f, 0.f, 0.f});
    f32x4 v;
#pragma unroll
    for (int k = 0; k < 4; ++k) { const float lo = (g & 2) ? d[k][1] : d[k][0], hi = (g & 2) ? d[4 + k][1] : d[4 + k][0]; v[k] = (g & 1) ? hi : lo; }
    const int to = WS_INBOX + ((g >> 1) * 128 + 16 * w + c16) * INBOX_B + IB_T3 + idx * 64 + (g & 1) * 32;
    st16(rws, gran2(v.x, v.y, tag), to, 0);
    st16(rws, gran2(v.z, v.w, tag), to + 16, 0);
  }
  STAMP(9);
  {
    const int ib = WS_INBOX + idx * INBOX_B + IB_T3;
    v4u v0 = ld16<SC1>(rws, tid * 32, ib), v1 = ld16<SC1>(rws, tid * 32 + 16, ib);
    unsigned bad = bad2(v0, tag) | bad2(v1, tag);
    unsigned spin = 0;
    for (; !__all(bad == 0) && spin < SPIN_CAP; ++spin) { MEMFENCE(); v0 = ld16<SC1>(rws, tid * 32, ib); v1 = ld16<SC1>(rws, tid * 32 + 16, ib); bad = bad2(v0, tag) | bad2(v1, tag); }
    STAMP(10);
    COUNT(14, spin);
    f32x4 v = {dec(v0.x), dec(v0.z), dec(v1.x), dec(v1.z)};
#pragma unroll
    for (int k = 0; k < 4; ++k) { float z = v[k]; z += dpp<XOR2>(z); z += dpp<ROR4>(z); z += dpp<ROR8>(z); v[k] = x16_sum(z); }
    if (lane < 2) *(LAS f32x4*)(L + L_T3 + w * 32 + lane * 16) = v;
  }
  BAR();
  if (tid < 4) {
    float a0 = 0.f, a1 = 0.f;
#pragma unroll
    for (int ww = 0; ww < 8; ++ww) { a0 += *(LAS float*)(L + L_T3 + ww * 32 + tid * 8); a1 += *(LAS float*)(L + L_T3 + ww * 32 + tid * 8 + 4); }
    ((unsigned*)p.out)[idx * 4 + tid] = cvtpk(a0, a1);
    if (idx == 0 && tid == 0) __builtin_amdgcn_raw_buffer_store_b64(v2u{tag.lo, tag.hi}, rws, WS_EPOCH, 0, SC1);
  }
  while (__builtin_amdgcn_s_memrealtime() - t_begin < PAD_US * 100ull) __builtin_amdgcn_s_sleep(4);
}
}

extern "C" void kernel_launch(void* const* d_in, const int* in_sizes, int n_in, void* d_out, int out_size, void* d_ws, size_t ws_size,
                              hipStream_t stream) {
  Params p;
  p.x = d_in[0]; p.ak = d_in[1]; p.av = d_in[2]; p.wqa = d_in[3]; p.woa = d_in[4];
  p.bk = d_in[6]; p.bv = d_in[7]; p.wqb = d_in[8]; p.wob = d_in[9]; p.gw = d_in[10];
  p.out = d_out; p.ws = d_ws;
  hipLaunchKernelGGL(mix_decode, dim3(NWG), dim3(NTHR), 0, stream, p);
}
```

```cpp
#include <hip/hip_runtime.h>
#include <hip/hip_bf16.h>

#define TAU 113
#define LAGM 30
#define FLIGHT 4
#define CODEPF 1
constexpr int CODE_BYTES = 16640;

namespace {
typedef unsigned v4u __attribute__((ext_vector_type(4)));
typedef unsigned v2u __attribute__((ext_vector_type(2)));
typedef float f32x4 __attribute__((ext_vector_type(4)));
typedef float f32x2 __attribute__((ext_vector_type(2)));
typedef short bf16x8 __attribute__((ext_vector_type(8)));
typedef short bf16x4 __attribute__((ext_vector_type(4)));
typedef unsigned long long u64;
#define LAS __attribute__((address_space(3)))
typedef __amdgpu_buffer_rsrc_t rsrc_t;

constexpr int NWG = 256, NTHR = 512;
constexpr int SK = 32768, WIN = 4096;
constexpr int SC1 = 16, NT = 2;
constexpr int RW3 = 0x00020000;
constexpr int WS_EPOCH = 0;
constexpr int WS_QP = 4096;
constexpr int WS_ML = WS_QP + 131072;
constexpr int WS_O = WS_ML + 65536;
constexpr int WS_T3 = WS_O + 64 * 32768;
constexpr int WS_END = WS_T3 + 256 * 2048;
constexpr int WS_BYTES = WS_END;
constexpr int L_XQ = 0;
constexpr int L_Q = L_XQ + 64 * 144;
constexpr int L_MA = L_Q + 16 * 272;
constexpr int L_MB = L_MA + 256 * 144;
constexpr int L_MXA = L_MB + 256 * 144;
constexpr int L_LSA = L_MXA + 256;
constexpr int L_MXB = L_LSA + 256;
constexpr int L_LSB = L_MXB + 256;
constexpr int L_T2 = L_LSB + 256;
constexpr int L_T2B = L_T2 + 4096;
constexpr int L_GATE = L_T2B + 8192;
constexpr int L_END = L_GATE + 16;
constexpr float SCALE = 0.08838834764831845f;
constexpr float CS = 1.4426950408889634f;
constexpr unsigned SPIN_CAP = 1u << 16;

struct Params {
  const void* x; const void* ak; const void* av; const void* wqa; const void* woa;
  const void* bk; const void* bv; const void* wqb; const void* wob; const void* gw;
  void* out; void* ws;
};

__device__ __forceinline__ rsrc_t mk(const void* p, unsigned bytes) { return __builtin_amdgcn_make_buffer_rsrc((void*)p, 0, (int)bytes, RW3); }
template <int AUX> __device__ __forceinline__ v4u ld16(rsrc_t r, int voff, int soff) { return __builtin_amdgcn_raw_buffer_load_b128(r, voff, soff, AUX); }
__device__ __forceinline__ void st16(rsrc_t r, v4u v, int voff, int soff) { __builtin_amdgcn_raw_buffer_store_b128(v, r, voff, soff, SC1); }
__device__ __forceinline__ f32x4 mfma32(v4u a, v4u b, f32x4 c) {
  return __builtin_amdgcn_mfma_f32_16x16x32_bf16(__builtin_bit_cast(bf16x8, a), __builtin_bit_cast(bf16x8, b), c, 0, 0, 0);
}
__device__ __forceinline__ f32x4 mfma16(v2u a, v2u b, f32x4 c) {
  return __builtin_amdgcn_mfma_f32_16x16x16bf16_1k(__builtin_bit_cast(bf16x4, a), __builtin_bit_cast(bf16x4, b), c, 0, 0, 0);
}
__device__ __forceinline__ unsigned permlo(unsigned hi, unsigned lo) { return __builtin_amdgcn_perm(hi, lo, 0x05040100u); }
__device__ __forceinline__ unsigned permhi(unsigned hi, unsigned lo) { return __builtin_amdgcn_perm(hi, lo, 0x07060302u); }
__device__ __forceinline__ v4u col8(const v4u& r0, const v4u& r1, const v4u& r2, const v4u& r3, const v4u& r4, const v4u& r5, const v4u& r6, const v4u& r7, int t) {
  const int d = t >> 1;
  v4u o;
  if (t & 1) { o.x = permhi(r1[d], r0[d]); o.y = permhi(r3[d], r2[d]); o.z = permhi(r5[d], r4[d]); o.w = permhi(r7[d], r6[d]); }
  else       { o.x = permlo(r1[d], r0[d]); o.y = permlo(r3[d], r2[d]); o.z = permlo(r5[d], r4[d]); o.w = permlo(r7[d], r6[d]); }
  return o;
}
__device__ __forceinline__ v2u col4(const v4u& r0, const v4u& r1, const v4u& r2, const v4u& r3, int t) {
  const int d = t >> 1;
  v2u o;
  if (t & 1) { o.x = permhi(r1[d], r0[d]); o.y = permhi(r3[d], r2[d]); }
  else       { o.x = permlo(r1[d], r0[d]); o.y = permlo(r3[d], r2[d]); }
  return o;
}
__device__ __forceinline__ unsigned cvtpk(float lo, float hi) {
  typedef __bf16 b2 __attribute__((ext_vector_type(2)));
  f32x2 f = {lo, hi};
  return __builtin_bit_cast(unsigned, __builtin_convertvector(f, b2));
}
template <int CTRL> __device__ __forceinline__ float dpp(float x) {
  return __builtin_bit_cast(float, __builtin_amdgcn_mov_dpp(__builtin_bit_cast(int, x), CTRL, 0xf, 0xf, true));
}
constexpr int XOR1 = 0xB1, XOR2 = 0x4E, ROR4 = 0x124, ROR8 = 0x128;
__device__ __forceinline__ float sw16(float a, float b) {
  auto s = __builtin_amdgcn_permlane16_swap(__float_as_uint(a), __float_as_uint(b), false, false);
  return __uint_as_float(s[0]) + __uint_as_float(s[1]);
}
__device__ __forceinline__ float sw32(float a, float b) {
  auto s = __builtin_amdgcn_permlane32_swap(__float_as_uint(a), __float_as_uint(b), false, false);
  return __uint_as_float(s[0]) + __uint_as_float(s[1]);
}
__device__ __forceinline__ float max16(float x) {
  auto s = __builtin_amdgcn_permlane16_swap(__float_as_uint(x), __float_as_uint(x), false, false);
  return fmaxf(__uint_as_float(s[0]), __uint_as_float(s[1]));
}
__device__ __forceinline__ float x16_max(float x) {
  x = max16(x);
  auto t = __builtin_amdgcn_permlane32_swap(__float_as_uint(x), __float_as_uint(x), false, false);
  return fmaxf(__uint_as_float(t[0]), __uint_as_float(t[1]));
}
__device__ __forceinline__ float x16_sum(float x) { x = sw16(x, x); return sw32(x, x); }
__device__ __forceinline__ float ex2(float x) { return __builtin_amdgcn_exp2f(x); }
struct Tag { unsigned s0, s1, s2, s3, t0, t1, t2, t3; };
__device__ __forceinline__ unsigned enc(float f, unsigned sl) { return __builtin_amdgcn_alignbit(sl, __float_as_uint(f) + 0x800u, 12); }
__device__ __forceinline__ v4u gran(float a, float b, float c, float d, Tag t) { return v4u{enc(a, t.s0), enc(b, t.s1), enc(c, t.s2), enc(d, t.s3)}; }
__device__ __forceinline__ unsigned chk(unsigned acc, v4u v, Tag t) { return ((((v.x ^ t.t0) | acc) | (v.y ^ t.t1)) | (v.z ^ t.t2)) | (v.w ^ t.t3); }
__device__ __forceinline__ unsigned bad4(v4u v, Tag t) { return chk(0u, v, t) >> 20; }
__device__ __forceinline__ float dec(unsigned d) { return __uint_as_float(d << 12); }
#define BAR() asm volatile("s_waitcnt lgkmcnt(0)\n\ts_barrier" ::: "memory")
#define RAWBAR() asm volatile("s_barrier" ::: "memory")
#define SB() __builtin_amdgcn_sched_barrier(0)
#define THR_() do { if (!slowcu && !behind) asm volatile("s_waitcnt vmcnt(%0)" :: "n"(FLIGHT - 1)); SB(); } while (0)
#define MEMFENCE() asm volatile("" ::: "memory")

__global__ void __launch_bounds__(NTHR, 2) mix_decode(Params p) {
  __shared__ __attribute__((aligned(16))) unsigned char smem[L_END];
  LAS unsigned char* L = (LAS unsigned char*)smem;
  const int tid = threadIdx.x, lane = tid & 63, w = __builtin_amdgcn_readfirstlane(tid >> 6);
  const int g = lane >> 4, c16 = lane & 15;
  const int c = blockIdx.x, grp = c & 7, kvh = grp >> 1, b = grp & 1, s = c >> 3;
  const int i = b * 32 + s;
  const unsigned t_start = (unsigned)__builtin_amdgcn_s_memrealtime();
  bool behind = false;
#define PACE(pos) do { { unsigned el_ = (unsigned)__builtin_amdgcn_s_memrealtime() - t_start; behind = el_ > (unsigned)((pos) * TAU + LAGM); \
    while (el_ < (unsigned)((pos) * TAU)) { __builtin_amdgcn_s_sleep(1); el_ = (unsigned)__builtin_amdgcn_s_memrealtime() - t_start; } SB(); } } while (0)
  const rsrc_t rws = mk(p.ws, WS_BYTES);
  const rsrc_t rx = mk(p.x, 4096);
  const int cg = s >> 1, kh = s & 1;
  const bool slowcu = (cg & 7) == 3;
  v4u wq[8], wo[8];
  {
    const rsrc_t rq = mk(b ? p.wqb : p.wqa, 1024 * 4096 * 2);
    const int vo = (32 * (c16 >> 3) + 8 * g) * 8192 + (c16 & 7) * 16;
    const int so = (kh * 512 + 64 * w) * 8192 + (kvh * 1024 + cg * 64) * 2;
#pragma unroll
    for (int j = 0; j < 8; ++j) { wq[j] = ld16<NT>(rq, vo, so + j * 8192); SB(); }
  }
  SB();
  const v2u ep = __builtin_amdgcn_raw_buffer_load_b64(rws, WS_EPOCH, 0, SC1);
  const v4u xf = ld16<0>(rx, (c16 & 1) * 2048 + (kh * 512 + 64 * w + 32 * ((c16 >> 1) & 1) + 8 * g) * 2, 0);
  unsigned code_pf = 0;
  if (CODEPF && w == 5 && lane < 5 && (s * 5 + lane) * 128 < CODE_BYTES) {
    unsigned long long pc; asm volatile("s_getpc_b64 %0" : "=s"(pc));
    code_pf = *(const __attribute__((address_space(1))) unsigned*)((pc & ~127ull) + (unsigned)(s * 5 + lane) * 128u);
  }
  v4u gx[6];
  if (w == 7) {
    const rsrc_t rg = mk(p.gw, 2048);
    gx[0] = ld16<0>(rx, lane * 16, 0); gx[1] = ld16<0>(rx, lane * 16, 1024);
    gx[2] = ld16<0>(rx, lane * 16, 2048); gx[3] = ld16<0>(rx, lane * 16, 3072);
    gx[4] = ld16<0>(rg, lane * 16, 0); gx[5] = ld16<0>(rg, lane * 16, 1024);
  }
  SB();
  RAWBAR();
  const int hc = s >> 2, nq = s & 3;
  const rsrc_t ro = mk(b ? p.wob : p.woa, 4096 * 1024 * 2);
  const int wo_vo = 2 * g * 2048 + c16 * 16;
  const int wo_so = ((kvh * 8 + hc) * 128 + (w >> 1) * 32) * 2048 + nq * 512 + (w & 1) * 256;
  const unsigned gbase = (unsigned)(b * 4 + kvh) * (SK * 256);
  const rsrc_t rak = mk(p.ak, 2u * 4 * SK * 256), rav = mk(p.av, 2u * 4 * SK * 256);
  const rsrc_t rbk = mk(p.bk, 2u * 4 * SK * 256), rbv = mk(p.bv, 2u * 4 * SK * 256);
  const int k_vo = (4 * (c16 & 3) + ((c16 >> 2) & 1)) * 256 + (c16 >> 3) * 64 + g * 16;
  const int v_vo = lane * 16;
  const int a_so = gbase + (SK - WIN + s * 128 + w * 16) * 256;
  const int b_so = gbase + (s * 1024 + w * 16) * 256;
  v4u akf[4], avf[4], kf[8][4], vf[8][4];
#define KIMM(j) (((j) >> 1) * 512 + ((j) & 1) * 128)
#define ISSUE_AK() do { _Pragma("unroll") for (int j_ = 0; j_ < 4; ++j_) { THR_(); akf[j_] = ld16<NT>(rak, k_vo + KIMM(j_), a_so); SB(); } } while (0)
#define ISSUE_AV() do { _Pragma("unroll") for (int j_ = 0; j_ < 4; ++j_) { THR_(); avf[j_] = ld16<NT>(rav, v_vo + j_ * 1024, a_so); SB(); } } while (0)
#define ISSUE_B(n) do { if ((n) >= 2) PACE(4 + (n)); if ((n) < 8) { _Pragma("unroll") for (int j_ = 0; j_ < 4; ++j_) { THR_(); kf[(n) & 7][j_] = ld16<NT>(rbk, k_vo + KIMM(j_), b_so + ((n) & 7) * 32768); SB(); } } \
    else { _Pragma("unroll") for (int j_ = 0; j_ < 4; ++j_) { THR_(); vf[(n) & 7][j_] = ld16<NT>(rbv, v_vo + j_ * 1024, b_so + ((n) & 7) * 32768); SB(); } } \
    SB(); } while (0)
  constexpr int PRE0 = 2, PRET = 4;
  ISSUE_AK();
  Tag tag;
  { const u64 e = ((((u64)ep.y << 32) | ep.x) + 0x001001001001ull); tag.s0 = (unsigned)e & 0xFFFu; tag.s1 = (unsigned)(e >> 12) & 0xFFFu; tag.s2 = (unsigned)(e >> 24) & 0xFFFu; tag.s3 = (unsigned)(e >> 36) & 0xFFFu;
    tag.s0 = __builtin_amdgcn_readfirstlane(tag.s0); tag.s1 = __builtin_amdgcn_readfirstlane(tag.s1); tag.s2 = __builtin_amdgcn_readfirstlane(tag.s2); tag.s3 = __builtin_amdgcn_readfirstlane(tag.s3);
    tag.t0 = tag.s0 << 20; tag.t1 = tag.s1 << 20; tag.t2 = tag.s2 << 20; tag.t3 = tag.s3 << 20; }
  {
    LAS unsigned char* dst = L + L_XQ + (((w * 2 + (g >> 1)) * 2 + (c16 & 1)) * 2 + (g & 1)) * 144;
    const bool wr = c16 < 4 && ((c16 >> 1) == (g >> 1));
#pragma unroll
    for (int t = 0; t < 8; ++t) {
      const v4u a = col8(wq[0], wq[1], wq[2], wq[3], wq[4], wq[5], wq[6], wq[7], t);
      const f32x4 d = mfma32(a, xf, f32x4{0.f, 0.f, 0.f, 0.f});
      if (wr) *(LAS f32x4*)(dst + t * 16) = d;
    }
  }
  BAR();
  asm volatile("" :: "v"(code_pf));
  if (w == 0) {
    const int o32 = lane & 31, ph = lane >> 5;
    const int bb = o32 >> 4, half = (o32 >> 3) & 1, t = o32 & 7;
    LAS unsigned char* src = L + L_XQ + ((ph * 8 * 2 + bb) * 2 + half) * 144 + t * 16;
    f32x4 r[8];
#pragma unroll
    for (int k = 0; k < 8; ++k) r[k] = *(LAS f32x4*)(src + k * 576);
    SB();
    f32x4 a = ((r[0] + r[1]) + (r[2] + r[3])) + ((r[4] + r[5]) + (r[6] + r[7]));
#pragma unroll
    for (int k = 0; k < 4; ++k) a[k] = sw32(a[k], a[k]);
    if (lane < 32) st16(rws, gran(a.x, a.y, a.z, a.w, tag), WS_QP + ((((kvh * 2 + bb) * 2 + b) * 2 + kh) * 16 + cg) * 256 + (half * 8 + t) * 16, 0);
  }
  if (w == 7) {
    float s0 = 0.f, s1 = 0.f;
#pragma unroll
    for (int h = 0; h < 2; ++h)
#pragma unroll
      for (int d = 0; d < 4; ++d) {
        const float g0 = __uint_as_float(gx[4 + h][d] << 16), g1 = __uint_as_float(gx[4 + h][d] & 0xffff0000u);
        s0 = fmaf(__uint_as_float(gx[h][d] << 16), g0, s0); s0 = fmaf(__uint_as_float(gx[h][d] & 0xffff0000u), g1, s0);
        s1 = fmaf(__uint_as_float(gx[2 + h][d] << 16), g0, s1); s1 = fmaf(__uint_as_float(gx[2 + h][d] & 0xffff0000u), g1, s1);
      }
    s0 += dpp<XOR1>(s0); s0 += dpp<XOR2>(s0); s0 += dpp<ROR4>(s0); s0 += dpp<ROR8>(s0); s0 = x16_sum(s0);
    s1 += dpp<XOR1>(s1); s1 += dpp<XOR2>(s1); s1 += dpp<ROR4>(s1); s1 += dpp<ROR8>(s1); s1 = x16_sum(s1);
    if (lane < 2) {
      const float z = lane ? s1 : s0;
      const float gt = 1.0f / (1.0f + __expf(-z));
      *(LAS float*)(L + L_GATE + lane * 4) = b ? 1.0f - gt : gt;
    }
  }
  SB();
  ISSUE_AV();
#pragma unroll
  for (int n = 0; n < PRE0; ++n) ISSUE_B(n);
  RAWBAR();
  const int q_vo = WS_QP + ((kvh * 2 + b) * 2 + (tid >> 8)) * 8192 + (tid & 255) * 16;
  v4u q0, q1;
  q0 = ld16<SC1>(rws, q_vo, 0); q1 = ld16<SC1>(rws, q_vo, 4096);
  SB();
  RAWBAR();
#pragma unroll
  for (int n = PRE0; n < PRET; ++n) ISSUE_B(n);
  {
    unsigned bad = bad4(q0, tag) | bad4(q1, tag);
    unsigned spin = 0;
    for (; !__all(bad == 0) && spin < SPIN_CAP; ++spin) {
      MEMFENCE();
      if (bad) { q0 = ld16<SC1>(rws, q_vo, 0); q1 = ld16<SC1>(rws, q_vo, 4096); bad = bad4(q0, tag) | bad4(q1, tag); }
    }
    const int pos = tid & 255, blk = pos >> 4, half = (pos >> 3) & 1, t = pos & 7;
    const int col = blk * 64 + half * 32 + t;
    LAS unsigned char* dq = L + L_Q + ((tid >> 8) * 8 + (col >> 7)) * 272 + (col & 127) * 2;
    const unsigned p01 = cvtpk(dec(q0.x) + dec(q1.x), dec(q0.y) + dec(q1.y)), p23 = cvtpk(dec(q0.z) + dec(q1.z), dec(q0.w) + dec(q1.w));
    *(LAS unsigned short*)(dq) = (unsigned short)p01;
    *(LAS unsigned short*)(dq + 16) = (unsigned short)(p01 >> 16);
    *(LAS unsigned short*)(dq + 32) = (unsigned short)p23;
    *(LAS unsigned short*)(dq + 48) = (unsigned short)(p23 >> 16);
  }
  BAR();
  v4u qa[2], qb[2];
#pragma unroll
  for (int k = 0; k < 2; ++k) {
    qa[k] = *(LAS v4u*)(L + L_Q + (c16 & 7) * 272 + k * 128 + (c16 >> 3) * 64 + g * 16);
    qb[k] = *(LAS v4u*)(L + L_Q + (8 + (c16 & 7)) * 272 + k * 128 + (c16 >> 3) * 64 + g * 16);
  }
#define SCORES(dst, kk, qq) do { \
    const f32x4 d0_ = mfma32(kk[1], qq[1], mfma32(kk[0], qq[0], f32x4{0.f, 0.f, 0.f, 0.f})); \
    const f32x4 d1_ = mfma32(kk[3], qq[1], mfma32(kk[2], qq[0], f32x4{0.f, 0.f, 0.f, 0.f})); \
    _Pragma("unroll") for (int j_ = 0; j_ < 4; ++j_) { \
      auto s_ = __builtin_amdgcn_permlane32_swap(__float_as_uint(d0_[j_]), __float_as_uint(d1_[j_]), false, false); \
      dst[j_] = __uint_as_float(s_[0]) + dpp<ROR8>(__uint_as_float(s_[1])); } \
  } while (0)
#define ROUND2(dst) do { _Pragma("unroll") for (int j_ = 0; j_ < 4; j_ += 2) { \
      const unsigned a_ = cvtpk(dst[j_], dst[j_ + 1]); \
      const unsigned b_ = cvtpk(__uint_as_float(a_ << 16) * SCALE, __uint_as_float(a_ & 0xffff0000u) * SCALE); \
      dst[j_] = __uint_as_float(b_ << 16); dst[j_ + 1] = __uint_as_float(b_ & 0xffff0000u); } } while (0)
#define WG_MAX(LMX, m) do { if (lane < 8) *(LAS float*)(L + (LMX) + (lane * 8 + w) * 4) = (m); BAR(); \
    const f32x4 ma_ = *(LAS f32x4*)(L + (LMX) + (c16 & 7) * 32), mb_ = *(LAS f32x4*)(L + (LMX) + (c16 & 7) * 32 + 16); \
    (m) = fmaxf(fmaxf(fmaxf(ma_.x, ma_.y), fmaxf(ma_.z, ma_.w)), fmaxf(fmaxf(mb_.x, mb_.y), fmaxf(mb_.z, mb_.w))) * CS; } while (0)
#define PUBLISH_ML(LMX, LLS, BR) do { if (w == 0 && lane < 8) { \
    const f32x4 ma_ = *(LAS f32x4*)(L + (LMX) + lane * 32), mb_ = *(LAS f32x4*)(L + (LMX) + lane * 32 + 16); \
    const f32x4 la_ = *(LAS f32x4*)(L + (LLS) + lane * 32), lb_ = *(LAS f32x4*)(L + (LLS) + lane * 32 + 16); \
    const unsigned M_ = __float_as_uint(fmaxf(fmaxf(fmaxf(ma_.x, ma_.y), fmaxf(ma_.z, ma_.w)), fmaxf(fmaxf(mb_.x, mb_.y), fmaxf(mb_.z, mb_.w))) * CS); \
    const float L_ = ((la_.x + la_.y) + (la_.z + la_.w)) + ((lb_.x + lb_.y) + (lb_.z + lb_.w)); \
    const v4u g_ = {(M_ >> 16) | (tag.s0 << 20), (M_ & 0xFFFFu) | (tag.s1 << 20), enc(L_, tag.s2), tag.s3 << 20}; \
    st16(rws, g_, WS_ML + (((kvh * 2 + (BR)) * 8 + lane) * 64 + i) * 16, 0); } } while (0)
#define PUBLISH_O(LM, BR) do { \
    const int piece = tid >> 4, wh = (tid >> 3) & 1, ch = tid & 7; \
    LAS unsigned char* src_ = L + (LM) + (wh * 128 + piece) * 144 + ch * 16; \
    const f32x4 r0_ = *(LAS f32x4*)(src_), r1_ = *(LAS f32x4*)(src_ + 4608), r2_ = *(LAS f32x4*)(src_ + 9216), r3_ = *(LAS f32x4*)(src_ + 13824); \
    f32x4 o_ = (r0_ + r1_) + (r2_ + r3_); \
    o_.x += dpp<ROR8>(o_.x); o_.y += dpp<ROR8>(o_.y); o_.z += dpp<ROR8>(o_.z); o_.w += dpp<ROR8>(o_.w); \
    if (wh == 0) st16(rws, gran(o_.x, o_.y, o_.z, o_.w, tag), WS_O + (((kvh * 2 + (BR)) * 8 + (piece & 7)) * 64 + i) * 512 + (piece >> 3) * 128 + ch * 16, 0); } while (0)
  {
    f32x4 sa;
    SCORES(sa, akf, qa);
    SB();
    ISSUE_B(PRET);
    ROUND2(sa);
    float m = x16_max(fmaxf(fmaxf(sa.x, sa.y), fmaxf(sa.z, sa.w)));
    WG_MAX(L_MXA, m);
    const float p0 = ex2(fmaf(sa.x, CS, -m)), p1 = ex2(fmaf(sa.y, CS, -m)), p2 = ex2(fmaf(sa.z, CS, -m)), p3 = ex2(fmaf(sa.w, CS, -m));
    const float l = x16_sum((p0 + p1) + (p2 + p3));
    const v2u pf = {cvtpk(p0, p1), cvtpk(p2, p3)};
    LAS unsigned char* dst = L + L_MA + (w * 32 + g * 8 + (c16 & 7)) * 144;
#pragma unroll
    for (int t = 0; t < 8; ++t) {
      const f32x4 o = mfma16(col4(avf[0], avf[1], avf[2], avf[3], t), pf, f32x4{0.f, 0.f, 0.f, 0.f});
      if (c16 < 8) *(LAS f32x4*)(dst + t * 16) = o;
    }
    if (lane < 8) *(LAS float*)(L + L_LSA + (lane * 8 + w) * 4) = l;
    SB();
    ISSUE_B(PRET + 1);
  }
  f32x4 sb[8];
#pragma unroll
  for (int u = 0; u < 8; ++u) {
    SCORES(sb[u], kf[u], qb);
    SB();
    ISSUE_B(PRET + 2 + u);
    ROUND2(sb[u]);
    SB();
    if (u < 7) RAWBAR();
  }
  ISSUE_B(14);
  v4u pf[4];
  {
    float m = fmaxf(fmaxf(sb[0].x, sb[0].y), fmaxf(sb[0].z, sb[0].w));
#pragma unroll
    for (int u = 1; u < 8; ++u) m = fmaxf(m, fmaxf(fmaxf(sb[u].x, sb[u].y), fmaxf(sb[u].z, sb[u].w)));
    m = x16_max(m);
    WG_MAX(L_MXB, m);
    float l = 0.f;
#pragma unroll
    for (int tt = 0; tt < 4; ++tt) {
      float pp[8];
#pragma unroll
      for (int j = 0; j < 4; ++j) { pp[j] = ex2(fmaf(sb[2 * tt][j], CS, -m)); pp[4 + j] = ex2(fmaf(sb[2 * tt + 1][j], CS, -m)); }
      l += ((pp[0] + pp[1]) + (pp[2] + pp[3])) + ((pp[4] + pp[5]) + (pp[6] + pp[7]));
      pf[tt] = v4u{cvtpk(pp[0], pp[1]), cvtpk(pp[2], pp[3]), cvtpk(pp[4], pp[5]), cvtpk(pp[6], pp[7])};
    }
    l = x16_sum(l);
    if (lane < 8) *(LAS float*)(L + L_LSB + (lane * 8 + w) * 4) = l;
  }
  SB();
  ISSUE_B(15);
  PUBLISH_ML(L_MXA, L_LSA, 0); PUBLISH_O(L_MA, 0); SB();
  RAWBAR();
  f32x4 acc[8];
#pragma unroll
  for (int t = 0; t < 8; ++t) acc[t] = f32x4{0.f, 0.f, 0.f, 0.f};
#pragma unroll
  for (int tt = 0; tt < 4; ++tt) {
#pragma unroll
    for (int t = 0; t < 8; ++t)
      acc[t] = mfma32(col8(vf[2 * tt][0], vf[2 * tt][1], vf[2 * tt][2], vf[2 * tt][3], vf[2 * tt + 1][0], vf[2 * tt + 1][1], vf[2 * tt + 1][2], vf[2 * tt + 1][3], t), pf[tt], acc[t]);
    SB();
    if (tt == 1) { PACE(20); THR_(); wo[0] = ld16<NT>(ro, wo_vo, wo_so + 0 * 2048); SB(); THR_(); wo[1] = ld16<NT>(ro, wo_vo, wo_so + 8 * 2048); SB(); }
    if (tt == 2) { THR_(); wo[2] = ld16<NT>(ro, wo_vo, wo_so + 16 * 2048); SB(); THR_(); wo[3] = ld16<NT>(ro, wo_vo, wo_so + 24 * 2048); SB(); }
  }
  {
    LAS unsigned char* dst = L + L_MB + (w * 32 + g * 8 + (c16 & 7)) * 144;
    if (c16 < 8) {
#pragma unroll
      for (int t = 0; t < 8; ++t) *(LAS f32x4*)(dst + t * 16) = acc[t];
    }
  }
  BAR();
  PUBLISH_O(L_MB, 1);
  PUBLISH_ML(L_MXB, L_LSB, 1);
  SB();
  const int ml_vo = WS_ML + (((kvh * 2 + b) * 8 + hc) * 64 + (w >> 2) * 32 + (lane & 31)) * 16;
  const int o_vo = WS_O + (((kvh * 2 + b) * 8 + hc) * 64 + 8 * w + 2 * g) * 512 + ((lane >> 3) & 1) * 256 + (lane & 7) * 16;
  int o_vo_ = o_vo; asm volatile("" : "+v"(o_vo_));
  v4u mlq, o00, o01, o10, o11, wof[8];
#pragma unroll
  for (int t = 0; t < 8; ++t) { const v2u h = col4(wo[0], wo[1], wo[2], wo[3], t); wof[t].x = h.x; wof[t].y = h.y; asm volatile("" : "+v"(wof[t].x), "+v"(wof[t].y)); }
  SB();
  asm volatile("s_waitcnt vmcnt(0)");
  SB();
#pragma unroll
  for (int j = 4; j < 8; ++j) wo[j] = ld16<NT>(ro, wo_vo, wo_so + (8 * (j & 3) + 1) * 2048);
  SB();
  mlq = ld16<SC1>(rws, ml_vo, 0);
  o00 = ld16<SC1>(rws, o_vo_, 0); o01 = ld16<SC1>(rws, o_vo_ + 128, 0); o10 = ld16<SC1>(rws, o_vo_ + 512, 0); o11 = ld16<SC1>(rws, o_vo_ + 640, 0);
  SB();
  float coef0 = 0.f, coef1 = 0.f;
#define COEF() do { \
    const float m_ = __uint_as_float((mlq.x << 16) | (mlq.y & 0xFFFFu)), l_ = dec(mlq.z); \
    float M_ = fmaxf(m_, dpp<XOR1>(m_)); M_ = fmaxf(M_, dpp<XOR2>(M_)); M_ = fmaxf(M_, dpp<ROR4>(M_)); M_ = fmaxf(M_, dpp<ROR8>(M_)); M_ = max16(M_); \
    const float e_ = ex2(m_ - M_); float L_ = e_ * l_; \
    L_ += dpp<XOR1>(L_); L_ += dpp<XOR2>(L_); L_ += dpp<ROR4>(L_); L_ += dpp<ROR8>(L_); L_ = sw16(L_, L_); \
    const float c_ = *(LAS float*)(L + L_GATE + (w >> 2) * 4) * e_ * __builtin_amdgcn_rcpf(L_); \
    coef0 = __shfl(c_, (w & 3) * 8 + 2 * g); coef1 = __shfl(c_, (w & 3) * 8 + 2 * g + 1); } while (0)
#pragma unroll
  for (int t = 0; t < 8; ++t) { const v2u h = col4(wo[4], wo[5], wo[6], wo[7], t); wof[t].z = h.x; wof[t].w = h.y; asm volatile("" : "+v"(wof[t])); }
  SB();
  {
    COEF();
    SB();
    unsigned acc_ = chk(chk(chk(chk(chk(0u, mlq, tag), o00, tag), o01, tag), o10, tag), o11, tag);
    unsigned spin = 0;
    if (__builtin_expect(!__all(acc_ < (1u << 20)), 0)) {
      do {
        MEMFENCE();
        if (acc_ >= (1u << 20)) {
          mlq = ld16<SC1>(rws, ml_vo, 0);
          o00 = ld16<SC1>(rws, o_vo_, 0); o01 = ld16<SC1>(rws, o_vo_ + 128, 0); o10 = ld16<SC1>(rws, o_vo_ + 512, 0); o11 = ld16<SC1>(rws, o_vo_ + 640, 0);
          acc_ = chk(chk(chk(chk(chk(0u, mlq, tag), o00, tag), o01, tag), o10, tag), o11, tag);
        }
      } while (!__all(acc_ < (1u << 20)) && ++spin < SPIN_CAP);
      COEF();
    }
    float v0[4], v1[4];
#pragma unroll
    for (int k = 0; k < 4; ++k) { v0[k] = fmaf(coef1, dec(o10[k]), coef0 * dec(o00[k])); v1[k] = fmaf(coef1, dec(o11[k]), coef0 * dec(o01[k])); }
    float r[4];
#pragma unroll
    for (int k = 0; k < 4; ++k) r[k] = sw16(v0[k], v1[k]);
    const float z0 = sw32(r[0], r[2]), z1 = sw32(r[1], r[3]);
    *(LAS f32x2*)(L + L_T2 + w * 512 + (2 * ((lane >> 3) & 1) + (g & 1)) * 128 + (lane & 7) * 16 + (g >> 1) * 8) = f32x2{z0, z1};
  }
  BAR();
  {
    LAS unsigned char* src = L + L_T2 + (c16 & 1) * 2048 + (w >> 1) * 128 + g * 32;
    const f32x4 a0 = *(LAS f32x4*)(src), a1 = *(LAS f32x4*)(src + 512), a2 = *(LAS f32x4*)(src + 1024), a3 = *(LAS f32x4*)(src + 1536);
    const f32x4 b0 = *(LAS f32x4*)(src + 16), b1 = *(LAS f32x4*)(src + 528), b2 = *(LAS f32x4*)(src + 1040), b3 = *(LAS f32x4*)(src + 1552);
    const f32x4 oa = (a0 + a1) + (a2 + a3), ob = (b0 + b1) + (b2 + b3);
    const v4u of = {cvtpk(oa.x, oa.y), cvtpk(oa.z, oa.w), cvtpk(ob.x, ob.y), cvtpk(ob.z, ob.w)};
    f32x4 d[8];
#pragma unroll
    for (int t = 0; t < 8; ++t) d[t] = mfma32(of, wof[t], f32x4{0.f, 0.f, 0.f, 0.f});
    f32x4 v;
#pragma unroll
    for (int k = 0; k < 4; ++k) { const float lo = (g & 2) ? d[k][1] : d[k][0], hi = (g & 2) ? d[4 + k][1] : d[4 + k][0]; v[k] = (g & 1) ? hi : lo; }
    *(LAS f32x4*)(L + L_T2B + w * 1024 + lane * 16) = v;
  }
  BAR();
  if (w < 2) {
    LAS unsigned char* src = L + L_T2B + w * 1024 + lane * 16;
    const f32x4 v = (*(LAS f32x4*)(src) + *(LAS f32x4*)(src + 2048)) + (*(LAS f32x4*)(src + 4096) + *(LAS f32x4*)(src + 6144));
    st16(rws, gran(v.x, v.y, v.z, v.w, tag), WS_T3 + (nq * 64 + (b * 4 + kvh) * 8 + hc) * 2048 + (((g >> 1) * 32 + w * 16 + c16) * 2 + (g & 1)) * 16, 0);
  }
  if (w == 0) {
    const int j = kvh * 64 + i;
    const int to = WS_T3 + (((j >> 5) & 3) * 64 + (lane >> 1)) * 2048 + (((j >> 7) * 32 + (j & 31)) * 2 + (lane & 1)) * 16;
    constexpr int T1O = 32 * 2048;
    v4u t0 = ld16<SC1>(rws, to, 0), t1 = ld16<SC1>(rws, to, T1O);
    unsigned acc_ = chk(chk(0u, t0, tag), t1, tag);
    unsigned spin = 0;
    while (!__all(acc_ < (1u << 20)) && ++spin < SPIN_CAP) {
      MEMFENCE();
      if (acc_ >= (1u << 20)) { t0 = ld16<SC1>(rws, to, 0); t1 = ld16<SC1>(rws, to, T1O); acc_ = chk(chk(0u, t0, tag), t1, tag); }
    }
    float z[4];
#pragma unroll
    for (int k = 0; k < 4; ++k) { float y = dec(t0[k]) + dec(t1[k]); y += dpp<XOR2>(y); y += dpp<ROR4>(y); y += dpp<ROR8>(y); z[k] = y; }
    const float y = sw32(sw16(z[0], z[1]), sw16(z[2], z[3]));
    if (c16 < 2) { ((unsigned short*)p.out)[j * 8 + 4 * c16 + g] = (unsigned short)cvtpk(y, y); }
    if (j == 0 && lane == 0) __builtin_amdgcn_raw_buffer_store_b64(v2u{tag.s0 | (tag.s1 << 12) | (tag.s2 << 24), (tag.s2 >> 8) | (tag.s3 << 4)}, rws, WS_EPOCH, 0, SC1);
  }
}
}

extern "C" void kernel_launch(void* const* d_in, const int* in_sizes, int n_in, void* d_out, int out_size, void* d_ws, size_t ws_size,
                              hipStream_t stream) {
  Params p;
  p.x = d_in[0]; p.ak = d_in[1]; p.av = d_in[2]; p.wqa = d_in[3]; p.woa = d_in[4];
  p.bk = d_in[6]; p.bv = d_in[7]; p.wqb = d_in[8]; p.wob = d_in[9]; p.gw = d_in[10];
  p.out = d_out; p.ws = d_ws;
  hipLaunchKernelGGL(mix_decode, dim3(NWG), dim3(NTHR), 0, stream, p);
}
```
